```python
import torch

def _softmax(x, axis=-1):
    return torch.softmax(x, dim=axis)
def _gelu(x):
    return torch.nn.functional.gelu(x, approximate="tanh")
def _silu(x):
    return torch.nn.functional.silu(x)
def _sigmoid(x):
    return torch.sigmoid(x)
def _layernorm(x, axis=-1, eps=1e-5):
    m = x.mean(dim=axis, keepdim=True)
    v = x.var(dim=axis, keepdim=True, unbiased=False)
    return (x - m) / torch.sqrt(v + eps)
def _scatter_add(tgt, idx, src, axis=0):
    return tgt.clone().index_add_(axis, idx, src)
def _segment_sum(src, seg, n):
    out = torch.zeros((n,) + src.shape[1:], dtype=src.dtype)
    return out.index_add_(0, seg, src)
def _pad(x, pw):
    flat = tuple(v for pair in reversed(pw) for v in pair)
    return torch.nn.functional.pad(x, flat)

kernel_name = 'parallel_sum__softmax_full_mha__mixer_channel_mlp__prompt'

def setup_inputs():

    _w = (lambda a: (lambda t: t.to(torch.bfloat16) if t.is_floating_point() else t)(torch.as_tensor(a)))
    def _A_setup():
        B, S, D = 1, 2048, 640
        return {
            "x": _w((lambda *s: torch.randn(*s, dtype=torch.bfloat16) * 0.05)(B, S, D)),
            "wqkv": _w((lambda *s: torch.randn(*s, dtype=torch.bfloat16) * 0.05)(D, 3 * D)),
            "wo": _w((lambda *s: torch.randn(*s, dtype=torch.bfloat16) * 0.05)(D, D)),
        }
    _A_in = _A_setup()

    def _B_setup():
        B, S, D, F = 1, 2048, 640, 1536
        return {
            "x": _w((lambda *s: torch.randn(*s, dtype=torch.bfloat16) * 0.05)(B, S, D)),
            "w1": _w((lambda *s: torch.randn(*s, dtype=torch.bfloat16) * 3.2)(D, F) * (D ** -0.5)),
            "b1": _w((lambda *s: torch.randn(*s, dtype=torch.bfloat16) * 1.6)(F)),
            "w2": _w((lambda *s: torch.randn(*s, dtype=torch.bfloat16) * 0.1)(F, D) * (F ** -0.5)),
            "b2": _w((lambda *s: torch.randn(*s, dtype=torch.bfloat16) * 0.2)(D)),
        }
    _B_in = _B_setup()

    return {
        "A_x": _A_in["x"],
        "A_wqkv": _A_in["wqkv"],
        "A_wo": _A_in["wo"],
        "B_x": _B_in["x"],
        "B_w1": _B_in["w1"],
        "B_b1": _B_in["b1"],
        "B_w2": _B_in["w2"],
        "B_b2": _B_in["b2"],
    }

def reference(A_x, A_wqkv, A_wo, B_x, B_w1, B_b1, B_w2, B_b2):
    def _A_ref(x, wqkv, wo):
        B, S, D = x.shape
        H = 32; Dh = D // H
        qkv = (lambda x, *d: x.permute(*d))((x @ wqkv).reshape(B, S, 3, H, Dh), 2, 0, 3, 1, 4)
        q, k, v = qkv[0], qkv[1], qkv[2]
        scale = Dh ** -0.5
        scores = torch.einsum("bhqd,bhkd->bhqk", q, k) * scale
        p = _softmax(scores, axis=-1)
        o = torch.einsum("bhqk,bhkd->bhqd", p, v)
        return (lambda x, *d: x.permute(*d))(o, 0, 2, 1, 3).reshape(B, S, D) @ wo

    def _B_ref(x, w1, b1, w2, b2):
        y = _layernorm(x, axis=-1)
        h = _gelu(torch.einsum("bsd,df->bsf", y, w1) + b1)
        return torch.einsum("bsf,fd->bsd", h, w2) + b2

    x = A_x
    xn = _layernorm(x, axis=-1)
    return x + _A_ref(xn, A_wqkv, A_wo) + _B_ref(xn, B_w1, B_b1, B_w2, B_b2)
```

```cpp
#include <hip/hip_runtime.h>
#include <hip/hip_bf16.h>
#include <stdint.h>

typedef unsigned short u16;
typedef short bf16x8 __attribute__((ext_vector_type(8)));
typedef short s16x4 __attribute__((ext_vector_type(4)));
typedef float f32x16 __attribute__((ext_vector_type(16)));
typedef float f32x4 __attribute__((ext_vector_type(4)));
typedef unsigned u32x4 __attribute__((ext_vector_type(4)));
typedef unsigned u32x2 __attribute__((ext_vector_type(2)));
typedef __bf16 bf2 __attribute__((ext_vector_type(2)));
#define LDS_AS __attribute__((address_space(3)))
typedef LDS_AS const char* lds_cptr;

constexpr int S_ = 2048, D_ = 640, F_ = 1536;
constexpr float EPS = 1e-5f;
constexpr float QSCALE = 0.22360679774997896f * 1.4426950408889634f;

constexpr size_t WS_Q = 0;
constexpr size_t WS_KV = WS_Q + (size_t)32 * 2048 * 40;
constexpr size_t WS_ACT = WS_KV + (size_t)32 * 196608;
constexpr int ACT_KS = 136;

#define MFMA32(a, b, c) __builtin_amdgcn_mfma_f32_32x32x16_bf16(a, b, c, 0, 0, 0)

typedef float f32x2 __attribute__((ext_vector_type(2)));
__device__ __forceinline__ unsigned cvtpk(float lo, float hi) {
  return __builtin_bit_cast(unsigned, __builtin_convertvector((f32x2){lo, hi}, bf2));
}
__device__ __forceinline__ void glds16(unsigned voff, const void* sbase, unsigned ldsb) {
  unsigned sv;
  asm volatile("s_mov_b32 %0, m0\n\ts_mov_b32 m0, %3\n\ts_nop 0\n\tglobal_load_lds_dwordx4 %1, %2\n\ts_mov_b32 m0, %0"
               : "=&s"(sv) : "v"(voff), "s"(sbase), "s"(ldsb) : "memory");
}
__device__ __forceinline__ void glds4(unsigned voff, const void* sbase, unsigned ldsb) {
  unsigned sv;
  asm volatile("s_mov_b32 %0, m0\n\ts_mov_b32 m0, %3\n\ts_nop 0\n\tglobal_load_lds_dword %1, %2\n\ts_mov_b32 m0, %0"
               : "=&s"(sv) : "v"(voff), "s"(sbase), "s"(ldsb) : "memory");
}
__device__ __forceinline__ void glds16x3(unsigned voff, const void* sbase, unsigned ldsb) {
  unsigned sv;
  asm volatile("s_mov_b32 %0, m0\n\ts_mov_b32 m0, %3\n\ts_nop 0\n\t"
               "global_load_lds_dwordx4 %1, %2\n\t"
               "global_load_lds_dwordx4 %1, %2 offset:1024\n\t"
               "global_load_lds_dwordx4 %1, %2 offset:2048\n\t"
               "s_mov_b32 m0, %0"
               : "=&s"(sv) : "v"(voff), "s"(sbase), "s"(ldsb) : "memory");
}
template <int N> __device__ __forceinline__ void wait_vm() { asm volatile("s_waitcnt vmcnt(%0)" ::"i"(N) : "memory"); }
template <int N> __device__ __forceinline__ void wait_vm_bar() {
  asm volatile("s_waitcnt vmcnt(%0) lgkmcnt(0)\n\ts_barrier" ::"i"(N) : "memory");
}
__device__ __forceinline__ void bar_lds() { asm volatile("s_waitcnt lgkmcnt(0)\n\ts_barrier" ::: "memory"); }
__device__ __forceinline__ bf16x8 lds_b128(lds_cptr p) { return *(const LDS_AS bf16x8*)p; }
__device__ __forceinline__ s16x4 lds_tr(lds_cptr p) {
  return __builtin_amdgcn_ds_read_tr16_b64_v4i16((LDS_AS s16x4*)p);
}
__device__ __forceinline__ bf16x8 cat8(s16x4 a, s16x4 b) {
  return __builtin_shufflevector(a, b, 0, 1, 2, 3, 4, 5, 6, 7);
}
__device__ __forceinline__ float dot2(unsigned a, unsigned b, float c) {
  return __builtin_amdgcn_fdot2_f32_bf16(__builtin_bit_cast(bf2, a), __builtin_bit_cast(bf2, b), c, false);
}
__device__ __forceinline__ float xhalf_sum(float v) {
  auto rr = __builtin_amdgcn_permlane32_swap(__float_as_uint(v), __float_as_uint(v), false, false);
  return __uint_as_float(rr[0]) + __uint_as_float(rr[1]);
}
__device__ __forceinline__ float xhalf_max(float v) {
  auto rr = __builtin_amdgcn_permlane32_swap(__float_as_uint(v), __float_as_uint(v), false, false);
  return fmaxf(__uint_as_float(rr[0]), __uint_as_float(rr[1]));
}
__device__ __forceinline__ float gelu_tanh(float x) {
  const float c0 = -2.0f * 0.7978845608028654f * 1.4426950408889634f;
  const float c1 = c0 * 0.044715f;
  float t = fmaf(x * x, c1, c0);
  float e = __builtin_amdgcn_exp2f(x * t);
  return x * __builtin_amdgcn_rcpf(1.0f + e);
}

constexpr int K1_XB = 16384, K1_WB = 7 * 4096, K1_STG = K1_XB + K1_WB;
constexpr int K1_MEAN = 3 * K1_STG, K1_R1 = K1_MEAN + 512, K1_R12 = K1_R1 + 512, K1_CS = K1_R12 + 512, K1_B1 = K1_CS + 896;
constexpr int K1_LDS = K1_B1 + 256;

template <int ROLE>
__device__ __forceinline__ void k1_loop(f32x16 (&acc)[4], float& cs, float& sx, float& sxx, char* lds, unsigned lds0, int w, int lane,
                                        unsigned vx, unsigned vq0, unsigned vq1, unsigned vm0, unsigned vm1, const char* xg,
                                        const char* wq, const char* wm, int rot) {
  constexpr int NT = ROLE == 2 ? 3 : 4;
  constexpr int T0 = ROLE == 2 ? 4 : 0;
  constexpr int PER = ROLE == 2 ? 5 : 6;
  const int r = lane & 31, hh = lane >> 5, tt = w & 3;
  auto issue = [&](int s) {
    int ks = s + rot; if (ks >= 10) ks -= 10;
    const unsigned sb = lds0 + (s % 3) * K1_STG;
    const char* xb = xg + ks * 128;
    const char* qb = wq + (size_t)ks * 64 * 3840;
    const char* mb = wm + (size_t)ks * 64 * 3072;
    glds16(vx, xb, sb + w * 1024);
    glds16(vx, xb + 64 * 1280, sb + (w + 8) * 1024);
    glds16(vq0, qb, sb + K1_XB + w * 1024);
    glds16(vq1, qb, sb + K1_XB + (w + 8) * 1024);
    glds16(vm0, mb, sb + K1_XB + (w + 16) * 1024);
    if (ROLE != 2) glds16(vm1, mb, sb + K1_XB + (w + 24) * 1024);
  };
  issue(0); issue(1); issue(2);
  const lds_cptr xrow = (lds_cptr)lds + (tt * 32 + r) * 128;
  const int sw = (r >> 1) & 7;
  const lds_cptr wbase = (lds_cptr)lds + K1_XB + T0 * 4096 + (8 * hh + ((lane & 15) >> 2)) * 64 + ((lane >> 4) & 1) * 32 + (lane & 3) * 8;
#pragma unroll
  for (int s = 0; s < 10; ++s) {
    if (s == 0) wait_vm_bar<2 * PER>();
    else if (s < 9) wait_vm_bar<PER>();
    else wait_vm_bar<0>();
    if (s >= 1 && s + 2 < 10) issue(s + 2);
    const int so = (s % 3) * K1_STG;
#pragma unroll
    for (int ks = 0; ks < 4; ++ks) {
      bf16x8 xa = lds_b128(xrow + so + (((2 * ks + hh) ^ sw) << 4));
      if (ROLE == 2) {
        u32x4 xu = __builtin_bit_cast(u32x4, xa);
#pragma unroll
        for (int i = 0; i < 4; ++i) { sx = dot2(xu[i], 0x3f803f80u, sx); sxx = dot2(xu[i], xu[i], sxx); }
      }
#pragma unroll
      for (int t = 0; t < NT; ++t) {
        lds_cptr wp = wbase + so + t * 4096 + ks * 1024;
        bf16x8 wf = cat8(lds_tr(wp), lds_tr(wp + 256));
        if (ROLE == 1) acc[t] = MFMA32(xa, wf, acc[t]);
        else acc[t] = MFMA32(wf, xa, acc[t]);
        if (tt == t) {
          u32x4 wu = __builtin_bit_cast(u32x4, wf);
#pragma unroll
          for (int i = 0; i < 4; ++i) cs = dot2(wu[i], 0x3f803f80u, cs);
        }
      }
    }
  }
}

__global__ __launch_bounds__(512, 2) void k1_kernel(const u16* __restrict__ x, const u16* __restrict__ wqkv, const u16* __restrict__ w1,
                                                     const u16* __restrict__ b1, char* __restrict__ ws) {
  __shared__ __attribute__((aligned(16))) char lds[K1_LDS];
  const unsigned lds0 = (unsigned)(uintptr_t)(LDS_AS char*)lds;
  const int b = blockIdx.x, xcd = b & 7, i = b >> 3;
  const int p = (xcd & 1) * 8 + (i & 7), cb = (xcd >> 1) * 4 + (i >> 3);
  const int rot = ((i & 7) + ((i >> 3) * 5 + 1) / 2) % 10;
  const int tid = threadIdx.x, lane = tid & 63, w = __builtin_amdgcn_readfirstlane(tid >> 6);
  const int r = lane & 31, hh = lane >> 5, tt = w & 3;
  const int qt0 = cb < 12 ? cb * 4 : 48 + (cb - 12) * 3;
  const int nq = cb < 12 ? 4 : 3;
  const int xr = 8 * w + (lane >> 3);
  const unsigned vx = (unsigned)((p * 128 + xr) * 1280 + (((lane & 7) ^ ((xr >> 1) & 7)) << 4));
  const int jj = w & 3, tw = w >> 2;
  const int krow = 16 * jj + (lane >> 2);
  const int t1q = min(2 + tw, nq - 1);
  const unsigned vq0 = (unsigned)(krow * 3840 + (qt0 + tw) * 64 + (lane & 3) * 16);
  const unsigned vq1 = (unsigned)(krow * 3840 + (qt0 + t1q) * 64 + (lane & 3) * 16);
  const unsigned vm0 = (unsigned)(krow * 3072 + (3 * cb + tw) * 64 + (lane & 3) * 16);
  const unsigned vm1 = (unsigned)(krow * 3072 + (3 * cb + 2) * 64 + (lane & 3) * 16);
  if (w == 7) glds4((unsigned)(cb * 192 + min(lane, 47) * 4), b1, lds0 + K1_B1);

  f32x16 acc[4] = {};
  float cs = 0.f, sx = 0.f, sxx = 0.f;
  if (w >= 4) k1_loop<2>(acc, cs, sx, sxx, lds, lds0, w, lane, vx, vq0, vq1, vm0, vm1, (const char*)x, (const char*)wqkv, (const char*)w1, rot);
  else if (cb >= 10) k1_loop<1>(acc, cs, sx, sxx, lds, lds0, w, lane, vx, vq0, vq1, vm0, vm1, (const char*)x, (const char*)wqkv, (const char*)w1, rot);
  else k1_loop<0>(acc, cs, sx, sxx, lds, lds0, w, lane, vx, vq0, vq1, vm0, vm1, (const char*)x, (const char*)wqkv, (const char*)w1, rot);

  float* s_mean = (float*)(lds + K1_MEAN);
  float* s_r1 = (float*)(lds + K1_R1);
  float* s_r12 = (float*)(lds + K1_R12);
  float* s_cs = (float*)(lds + K1_CS);
  cs = xhalf_sum(cs);
  if (w < 7 && hh == 0) s_cs[w * 32 + r] = cs;
  if (w >= 4) {
    sx = xhalf_sum(sx); sxx = xhalf_sum(sxx);
    const float mean = sx * (1.0f / 640.0f);
    const float var = fmaxf(sxx * (1.0f / 640.0f) - mean * mean, 0.0f);
    const float r1 = 1.0f / sqrtf(var + EPS);
    const float r2 = 1.0f / sqrtf(var * r1 * r1 + EPS);
    if (hh == 0) { s_mean[tt * 32 + r] = mean; s_r1[tt * 32 + r] = r1; s_r12[tt * 32 + r] = r1 * r2; }
  }
  __syncthreads();
  const int blk = p * 4 + tt;
  if (w >= 4) {
    const float mean = s_mean[tt * 32 + r], rs = s_r12[tt * 32 + r];
    const float nm = -mean * rs;
    char* hp = ws + WS_ACT + ((size_t)blk * ACT_KS + 40 + cb * 6) * 1024 + lane * 16;
#pragma unroll
    for (int t = 0; t < 3; ++t) {
      unsigned pk[8];
#pragma unroll
      for (int g = 0; g < 4; ++g) {
        const f32x4 c4 = *(const f32x4*)(s_cs + (4 + t) * 32 + 8 * g + 4 * hh);
        const u32x2 bb = *(const u32x2*)(lds + K1_B1 + (t * 32 + 8 * g + 4 * hh) * 2);
        float bv[4] = {__uint_as_float(bb[0] << 16), __uint_as_float(bb[0] & 0xffff0000u), __uint_as_float(bb[1] << 16),
                       __uint_as_float(bb[1] & 0xffff0000u)};
        float v[4];
#pragma unroll
        for (int q = 0; q < 4; ++q) v[q] = gelu_tanh(fmaf(acc[t][4 * g + q], rs, fmaf(nm, c4[q], bv[q])));
        pk[2 * g] = cvtpk(v[0], v[1]); pk[2 * g + 1] = cvtpk(v[2], v[3]);
      }
      *(u32x4*)(hp + (t * 2) * 1024) = (u32x4){pk[0], pk[1], pk[2], pk[3]};
      *(u32x4*)(hp + (t * 2 + 1) * 1024) = (u32x4){pk[4], pk[5], pk[6], pk[7]};
    }
  } else if (cb >= 10) {
    f32x4 mn[4], rr[4];
#pragma unroll
    for (int g = 0; g < 4; ++g) { mn[g] = *(const f32x4*)(s_mean + tt * 32 + 8 * g + 4 * hh); rr[g] = *(const f32x4*)(s_r1 + tt * 32 + 8 * g + 4 * hh); }
#pragma unroll
    for (int t = 0; t < 4; ++t) {
      if (t < nq) {
        const int n = (qt0 + t) * 32 + r - 1280;
        const int head = n / 20, d = n - head * 20;
        const float c = s_cs[t * 32 + r];
        unsigned pk[8];
#pragma unroll
        for (int g = 0; g < 4; ++g) {
          float v[4];
#pragma unroll
          for (int q = 0; q < 4; ++q) v[q] = rr[g][q] * fmaf(-mn[g][q], c, acc[t][4 * g + q]);
          pk[2 * g] = cvtpk(v[0], v[1]); pk[2 * g + 1] = cvtpk(v[2], v[3]);
        }
        char* vp = ws + WS_KV + (size_t)head * 196608 + blk * 3072 + 1536 + (hh * 24 + d) * 16;
        *(u32x4*)(vp) = (u32x4){pk[0], pk[1], pk[2], pk[3]};
        *(u32x4*)(vp + 768) = (u32x4){pk[4], pk[5], pk[6], pk[7]};
        if (d == 19) {
          const u32x4 ones = {0x3f803f80u, 0x3f803f80u, 0x3f803f80u, 0x3f803f80u};
          *(u32x4*)(vp + 16) = ones;
          *(u32x4*)(vp + 768 + 16) = ones;
        }
      }
    }
  } else {
    const bool isq = cb < 5;
    const float mean = s_mean[tt * 32 + r], rs = s_r1[tt * 32 + r] * (isq ? QSCALE : 1.0f);
    const float nm = -mean * rs;
    const int token = blk * 32 + r;
#pragma unroll
    for (int t = 0; t < 4; ++t) {
#pragma unroll
      for (int g = 0; g < 4; ++g) {
        const f32x4 c4 = *(const f32x4*)(s_cs + t * 32 + 8 * g + 4 * hh);
        float v[4];
#pragma unroll
        for (int q = 0; q < 4; ++q) v[q] = fmaf(acc[t][4 * g + q], rs, nm * c4[q]);
        const u32x2 pk = {cvtpk(v[0], v[1]), cvtpk(v[2], v[3])};
        const int G = (qt0 + t) * 8 + 2 * g + hh - (isq ? 0 : 160);
        const int head = G / 5, gd = G - head * 5;
        if (isq) {
          *(u32x2*)(ws + WS_Q + ((size_t)head * 2048 + token) * 40 + gd * 8) = pk;
        } else {
          char* kp = ws + WS_KV + (size_t)head * 196608 + blk * 3072 + (gd >> 1) * 512 + r * 16 + (gd & 1) * 8;
          *(u32x2*)kp = pk;
          if (gd == 4) *(u32x2*)(kp + 8) = (u32x2){0x00003f80u, 0u};
        }
      }
    }
  }
}

constexpr int K2_STG = 24576;
constexpr int K2_LDS = 3 * K2_STG;

__global__ __launch_bounds__(512, 2) void k2_kernel(char* __restrict__ ws) {
  __shared__ __attribute__((aligned(16))) char lds[K2_LDS];
  const unsigned lds0 = (unsigned)(uintptr_t)(LDS_AS char*)lds;
  const int b = blockIdx.x, xcd = b & 7, i = b >> 3;
  const int head = xcd * 4 + (i & 3), qb = i >> 2;
  const int tid = threadIdx.x, lane = tid & 63, w = __builtin_amdgcn_readfirstlane(tid >> 6);
  const int r = lane & 31, hh = lane >> 5;
  const char* kvg = ws + WS_KV + (size_t)head * 196608;
  const unsigned vdma = (unsigned)(w * 3072 + lane * 16);
  glds16x3(vdma, kvg, lds0 + w * 3072);
  const int token = qb * 256 + w * 32 + r;
  const char* qp = ws + WS_Q + ((size_t)head * 2048 + token) * 40;
  const u32x2 qa = *(const u32x2*)(qp + hh * 16), qb2 = *(const u32x2*)(qp + hh * 16 + 8), qc = *(const u32x2*)(qp + 32);
  u32x4 q0u = {qa[0], qa[1], qb2[0], qb2[1]};
  u32x4 q1u = {hh ? 0u : qc[0], hh ? 0u : qc[1], 0u, 0u};
  asm volatile("" : "+v"(q0u), "+v"(q1u));
  glds16x3(vdma, kvg + K2_STG, lds0 + K2_STG + w * 3072);
  const bf16x8 q0 = __builtin_bit_cast(bf16x8, q0u);
  const int dcl = r <= 20 ? r : (r < 28 ? 20 : 16);
  const lds_cptr kp0 = (lds_cptr)lds + lane * 16;
  const lds_cptr kp1 = (lds_cptr)lds + 1024 + r * 16;
  const lds_cptr vp0 = (lds_cptr)lds + 1536 + (hh * 24 + dcl) * 16;
  const f32x16 zero = {};
  wait_vm_bar<3>();
  {
    f32x16 s = MFMA32(lds_b128(kp0), q0, zero);
    s = MFMA32(lds_b128(kp1), __builtin_bit_cast(bf16x8, q1u), s);
    float m = s[0];
#pragma unroll
    for (int q = 1; q < 16; ++q) m = fmaxf(m, s[q]);
    m = xhalf_max(m);
    q1u[2] = hh ? 0u : (cvtpk(-m, 0.0f) & 0xffffu);
  }
  const bf16x8 q1 = __builtin_bit_cast(bf16x8, q1u);
  f32x16 o = {};
  for (int st = 0; st < 8; ++st) {
    if (st > 0) { if (st < 7) wait_vm_bar<3>(); else wait_vm_bar<0>(); }
    if (st + 2 < 8) {
      const int sl = (st + 2) % 3;
      glds16x3(vdma, kvg + (st + 2) * K2_STG, lds0 + sl * K2_STG + w * 3072);
    }
    const int so = (st % 3) * K2_STG;
#pragma unroll
    for (int t = 0; t < 8; ++t) {
      const int to = so + t * 3072;
      f32x16 s = MFMA32(lds_b128(kp0 + to), q0, zero);
      s = MFMA32(lds_b128(kp1 + to), q1, s);
      unsigned pk[8];
#pragma unroll
      for (int q = 0; q < 8; ++q) pk[q] = cvtpk(__builtin_amdgcn_exp2f(s[2 * q]), __builtin_amdgcn_exp2f(s[2 * q + 1]));
      o = MFMA32(lds_b128(vp0 + to), __builtin_bit_cast(bf16x8, ((u32x4){pk[0], pk[1], pk[2], pk[3]})), o);
      o = MFMA32(lds_b128(vp0 + to + 768), __builtin_bit_cast(bf16x8, ((u32x4){pk[4], pk[5], pk[6], pk[7]})), o);
    }
  }
  float l;
  {
    auto rr = __builtin_amdgcn_permlane32_swap(__float_as_uint(o[8]), __float_as_uint(o[8]), false, false);
    l = __uint_as_float(rr[1]);
  }
  float amax = 0.f;
#pragma unroll
  for (int q = 0; q < 12; ++q) amax = fmaxf(amax, fabsf(o[q]));
  const bool bad = !(l < 1.0e9f) || !(l > 0.25f) || !(amax < 1.0e30f);
  if (__builtin_expect(__any(bad), 0)) {
    q1u[2] = 0u;
    const bf16x8 q1z = __builtin_bit_cast(bf16x8, q1u);
    float m = -INFINITY;
    o = zero;
    for (int t = 0; t < 64; ++t) {
      const char* tp = kvg + t * 3072;
      f32x16 s = MFMA32(*(const bf16x8*)(tp + lane * 16), q0, zero);
      s = MFMA32(*(const bf16x8*)(tp + 1024 + r * 16), q1z, s);
      float tm = s[0];
#pragma unroll
      for (int q = 1; q < 16; ++q) tm = fmaxf(tm, s[q]);
      tm = xhalf_max(tm);
      const float mn = fmaxf(m, tm);
      const float al = __builtin_amdgcn_exp2f(m - mn);
      m = mn;
      unsigned pk[8];
#pragma unroll
      for (int q = 0; q < 8; ++q) pk[q] = cvtpk(__builtin_amdgcn_exp2f(s[2 * q] - mn), __builtin_amdgcn_exp2f(s[2 * q + 1] - mn));
#pragma unroll
      for (int q = 0; q < 16; ++q) o[q] *= al;
      const char* vp = tp + 1536 + (hh * 24 + dcl) * 16;
      o = MFMA32(*(const bf16x8*)(vp), __builtin_bit_cast(bf16x8, ((u32x4){pk[0], pk[1], pk[2], pk[3]})), o);
      o = MFMA32(*(const bf16x8*)(vp + 768), __builtin_bit_cast(bf16x8, ((u32x4){pk[4], pk[5], pk[6], pk[7]})), o);
    }
    auto rr = __builtin_amdgcn_permlane32_swap(__float_as_uint(o[8]), __float_as_uint(o[8]), false, false);
    l = __uint_as_float(rr[1]);
  }
  const float il = 1.0f / l;
  char* op = ws + WS_ACT + (size_t)(qb * 8 + w) * ACT_KS * 1024;
#pragma unroll
  for (int g = 0; g < 3; ++g) {
    if (g < 2 || hh == 0) {
      const int G = head * 5 + 2 * g + hh;
      const u32x2 pk = {cvtpk(o[4 * g] * il, o[4 * g + 1] * il), cvtpk(o[4 * g + 2] * il, o[4 * g + 3] * il)};
      *(u32x2*)(op + (G >> 2) * 1024 + ((G & 1) * 32 + r) * 16 + ((G >> 1) & 1) * 8) = pk;
    }
  }
}

constexpr int K3_SLOT = 6144;
constexpr int K3_LDS = 8 * 3 * K3_SLOT;

__global__ __launch_bounds__(512, 2) void k3_kernel(const u16* __restrict__ x, const u16* __restrict__ wo, const u16* __restrict__ w2,
                                                     const u16* __restrict__ b2, const char* __restrict__ ws, u16* __restrict__ out) {
  __shared__ __attribute__((aligned(16))) char lds[K3_LDS];
  const unsigned lds0 = (unsigned)(uintptr_t)(LDS_AS char*)lds;
  const int b = blockIdx.x, xcd = b & 7, i = b >> 3;
  const int blk = xcd * 8 + (i & 7), cq = i >> 3;
  const int tid = threadIdx.x, lane = tid & 63, w = __builtin_amdgcn_readfirstlane(tid >> 6);
  const int r = lane & 31, hh = lane >> 5;
  const char* act = ws + WS_ACT + (size_t)blk * ACT_KS * 1024;
  const unsigned va = lane * 16;
  const unsigned vw = (unsigned)((lane >> 2) * 1280 + cq * 320 + (lane & 3) * 16);
  const unsigned myl = lds0 + w * 3 * K3_SLOT;
  auto issue = [&](int j) {
    const int ks = w * 17 + j;
    const unsigned sb = myl + (j % 3) * K3_SLOT;
    const char* wsrc = ks < 40 ? (const char*)wo + (size_t)ks * 16 * 1280 : (const char*)w2 + (size_t)(ks - 40) * 16 * 1280;
    glds16(va, act + ks * 1024, sb);
#pragma unroll
    for (int t = 0; t < 5; ++t) glds16(vw, wsrc + t * 64, sb + 1024 + t * 1024);
  };
  issue(0); issue(1); issue(2);
  const lds_cptr ap = (lds_cptr)lds + w * 3 * K3_SLOT + lane * 16;
  const lds_cptr wp0 = (lds_cptr)lds + w * 3 * K3_SLOT + 1024 + (4 * hh + ((lane & 15) >> 2)) * 64 + ((lane >> 4) & 1) * 32 + (lane & 3) * 8;
  f32x16 acc[5] = {};
#pragma unroll
  for (int j = 0; j < 17; ++j) {
    if (j < 15) wait_vm<12>(); else if (j == 15) wait_vm<6>(); else wait_vm<0>();
    const int so = (j % 3) * K3_SLOT;
    const bf16x8 af = lds_b128(ap + so);
    bf16x8 wf[5];
#pragma unroll
    for (int t = 0; t < 5; ++t) wf[t] = cat8(lds_tr(wp0 + so + t * 1024), lds_tr(wp0 + so + t * 1024 + 512));
    asm volatile("s_waitcnt lgkmcnt(0)" ::: "memory");
    if (j + 3 < 17) issue(j + 3);
#pragma unroll
    for (int t = 0; t < 5; ++t) acc[t] = MFMA32(wf[t], af, acc[t]);
  }
  constexpr int RP = 164;
  float* red = (float*)lds;
  __syncthreads();
  if (w >= 4) {
#pragma unroll
    for (int t = 0; t < 5; ++t)
#pragma unroll
      for (int g = 0; g < 4; ++g)
        *(f32x4*)(red + ((w - 4) * 32 + r) * RP + t * 32 + 8 * g + 4 * hh) = (f32x4){acc[t][4 * g], acc[t][4 * g + 1], acc[t][4 * g + 2], acc[t][4 * g + 3]};
  }
  __syncthreads();
  if (w < 4) {
#pragma unroll
    for (int t = 0; t < 5; ++t)
#pragma unroll
      for (int g = 0; g < 4; ++g) {
        float* pp = red + (w * 32 + r) * RP + t * 32 + 8 * g + 4 * hh;
        f32x4 v = *(f32x4*)pp;
        v += (f32x4){acc[t][4 * g], acc[t][4 * g + 1], acc[t][4 * g + 2], acc[t][4 * g + 3]};
        *(f32x4*)pp = v;
      }
  }
  __syncthreads();
  for (int it = tid; it < 32 * 20; it += 512) {
    const int tok = it / 20, c8 = it - tok * 20;
    f32x4 a0 = {}, a1 = {};
#pragma unroll
    for (int q = 0; q < 4; ++q) {
      const float* pp = red + (q * 32 + tok) * RP + c8 * 8;
      a0 += *(const f32x4*)pp; a1 += *(const f32x4*)(pp + 4);
    }
    const size_t go = (size_t)(blk * 32 + tok) * 640 + cq * 160 + c8 * 8;
    const u32x4 xv = *(const u32x4*)(x + go);
    const u32x4 bv = *(const u32x4*)(b2 + cq * 160 + c8 * 8);
    float v[8];
#pragma unroll
    for (int q = 0; q < 4; ++q) {
      const float lo = (q < 2 ? a0[2 * q] : a1[2 * q - 4]), hi = (q < 2 ? a0[2 * q + 1] : a1[2 * q - 3]);
      v[2 * q] = lo + __uint_as_float(xv[q] << 16) + __uint_as_float(bv[q] << 16);
      v[2 * q + 1] = hi + __uint_as_float(xv[q] & 0xffff0000u) + __uint_as_float(bv[q] & 0xffff0000u);
    }
    *(u32x4*)(out + go) = (u32x4){cvtpk(v[0], v[1]), cvtpk(v[2], v[3]), cvtpk(v[4], v[5]), cvtpk(v[6], v[7])};
  }
}

extern "C" void kernel_launch(void* const* d_in, const int* in_sizes, int n_in, void* d_out, int out_size, void* d_ws, size_t ws_size,
                              hipStream_t stream) {
  const u16* x = (const u16*)d_in[0];
  const u16* wqkv = (const u16*)d_in[1];
  const u16* wo = (const u16*)d_in[2];
  const u16* w1 = (const u16*)d_in[4];
  const u16* b1 = (const u16*)d_in[5];
  const u16* w2 = (const u16*)d_in[6];
  const u16* b2 = (const u16*)d_in[7];
  char* ws = (char*)d_ws;
  k1_kernel<<<256, 512, 0, stream>>>(x, wqkv, w1, b1, ws);
  k2_kernel<<<256, 512, 0, stream>>>(ws);
  k3_kernel<<<256, 512, 0, stream>>>(x, wo, w2, b2, ws, (u16*)d_out);
}
```

```cpp
#include <hip/hip_runtime.h>
#include <hip/hip_bf16.h>
#include <stdint.h>

typedef unsigned short u16;
typedef short bf16x8 __attribute__((ext_vector_type(8)));
typedef short s16x4 __attribute__((ext_vector_type(4)));
typedef float f32x16 __attribute__((ext_vector_type(16)));
typedef float f32x4 __attribute__((ext_vector_type(4)));
typedef float f32x2 __attribute__((ext_vector_type(2)));
typedef unsigned u32x4 __attribute__((ext_vector_type(4)));
typedef unsigned u32x2 __attribute__((ext_vector_type(2)));
typedef __bf16 bf2 __attribute__((ext_vector_type(2)));
#define LDS_AS __attribute__((address_space(3)))
typedef LDS_AS const char* lds_cptr;

constexpr float EPS = 1e-5f;
constexpr float QSCALE = 0.22360679774997896f * 1.4426950408889634f;

constexpr size_t WS_Q = 0;
constexpr size_t WS_KV = WS_Q + (size_t)32 * 2048 * 40;
constexpr size_t WS_O = WS_KV + (size_t)32 * 196608;
constexpr size_t WS_H = WS_O + (size_t)64 * 40 * 1024;
constexpr size_t WS_XN = WS_H + (size_t)64 * 96 * 1024;
constexpr size_t WS_R2 = WS_XN + (size_t)2048 * 1280;
constexpr size_t WS_WOT = WS_R2 + 8192;

#define MFMA32(a, b, c) __builtin_amdgcn_mfma_f32_32x32x16_bf16(a, b, c, 0, 0, 0)
#define SB() __builtin_amdgcn_sched_barrier(0)
#define EXP2(v) __builtin_amdgcn_exp2f(v)
#define PK4(a, b, c, d) __builtin_bit_cast(bf16x8, ((u32x4){a, b, c, d}))

#define STAMP(i)

__device__ __forceinline__ unsigned cvtpk(float lo, float hi) {
  return __builtin_bit_cast(unsigned, __builtin_convertvector((f32x2){lo, hi}, bf2));
}
__device__ __forceinline__ float bflo(unsigned u) { return __uint_as_float(u << 16); }
__device__ __forceinline__ float bfhi(unsigned u) { return __uint_as_float(u & 0xffff0000u); }
__device__ __forceinline__ void glds16(unsigned voff, const void* sbase, unsigned ldsb) {
  unsigned sv;
  asm volatile("s_mov_b32 %0, m0\n\ts_mov_b32 m0, %3\n\ts_nop 0\n\tglobal_load_lds_dwordx4 %1, %2\n\ts_mov_b32 m0, %0"
               : "=&s"(sv) : "v"(voff), "s"(sbase), "s"(ldsb) : "memory");
}
__device__ __forceinline__ void glds4(unsigned voff, const void* sbase, unsigned ldsb) {
  unsigned sv;
  asm volatile("s_mov_b32 %0, m0\n\ts_mov_b32 m0, %3\n\ts_nop 0\n\tglobal_load_lds_dword %1, %2\n\ts_mov_b32 m0, %0"
               : "=&s"(sv) : "v"(voff), "s"(sbase), "s"(ldsb) : "memory");
}
__device__ __forceinline__ void glds16x3(unsigned voff, const void* sbase, unsigned ldsb) {
  unsigned sv;
  asm volatile("s_mov_b32 %0, m0\n\ts_mov_b32 m0, %3\n\ts_nop 0\n\t"
               "global_load_lds_dwordx4 %1, %2\n\t"
               "global_load_lds_dwordx4 %1, %2 offset:1024\n\t"
               "global_load_lds_dwordx4 %1, %2 offset:2048\n\t"
               "s_mov_b32 m0, %0"
               : "=&s"(sv) : "v"(voff), "s"(sbase), "s"(ldsb) : "memory");
}
template <int N> __device__ __forceinline__ void wait_vm() { asm volatile("s_waitcnt vmcnt(%0)" ::"i"(N) : "memory"); }
template <int N> __device__ __forceinline__ void wait_vm_bar() {
  asm volatile("s_waitcnt vmcnt(%0) lgkmcnt(0)\n\ts_barrier" ::"i"(N) : "memory");
}
__device__ __forceinline__ void bar_lds() { asm volatile("s_waitcnt lgkmcnt(0)\n\ts_barrier" ::: "memory"); }
__device__ __forceinline__ void bar_raw() { asm volatile("s_barrier" ::: "memory"); }
__device__ __forceinline__ bf16x8 lds_b128(lds_cptr p) { return *(const LDS_AS bf16x8*)p; }
__device__ __forceinline__ s16x4 lds_tr(lds_cptr p) { return __builtin_amdgcn_ds_read_tr16_b64_v4i16((LDS_AS s16x4*)p); }
__device__ __forceinline__ bf16x8 cat8(s16x4 a, s16x4 b) { return __builtin_shufflevector(a, b, 0, 1, 2, 3, 4, 5, 6, 7); }
__device__ __forceinline__ float xhalf_max(float v) {
  auto rr = __builtin_amdgcn_permlane32_swap(__float_as_uint(v), __float_as_uint(v), false, false);
  return fmaxf(__uint_as_float(rr[0]), __uint_as_float(rr[1]));
}
__device__ __forceinline__ float upper_half(float v) {
  auto rr = __builtin_amdgcn_permlane32_swap(__float_as_uint(v), __float_as_uint(v), false, false);
  return __uint_as_float(rr[1]);
}
__device__ __forceinline__ float gelu_tanh(float x) {
  const float c0 = -2.0f * 0.7978845608028654f * 1.4426950408889634f;
  const float c1 = c0 * 0.044715f;
  float t = fmaf(x * x, c1, c0);
  float e = __builtin_amdgcn_exp2f(x * t);
  return x * __builtin_amdgcn_rcpf(1.0f + e);
}
__device__ __forceinline__ float wave_sum(float v) {
#pragma unroll
  for (int o = 32; o >= 1; o >>= 1) v += __shfl_xor(v, o);
  return v;
}

__global__ __launch_bounds__(512) void k0_kernel(const u16* __restrict__ x, char* __restrict__ ws) {
  const int lane = threadIdx.x & 63, row = blockIdx.x * 8 + (threadIdx.x >> 6);
  const unsigned* xp = (const unsigned*)(x + (size_t)row * 640);
  unsigned v[5];
#pragma unroll
  for (int i = 0; i < 5; ++i) v[i] = xp[i * 64 + lane];
  float s = 0.f;
#pragma unroll
  for (int i = 0; i < 5; ++i) s += bflo(v[i]) + bfhi(v[i]);
  const float mean = wave_sum(s) * (1.0f / 640.0f);
  float q = 0.f;
#pragma unroll
  for (int i = 0; i < 5; ++i) { const float a = bflo(v[i]) - mean, b = bfhi(v[i]) - mean; q += a * a + b * b; }
  const float var = wave_sum(q) * (1.0f / 640.0f);
  const float r1 = __builtin_amdgcn_rsqf(var + EPS);
  const int rr = row & 127;
  char* op = ws + WS_XN + (size_t)(row >> 7) * 163840 + rr * 128 + ((((lane >> 2) & 7) ^ ((rr >> 1) & 7)) << 4) + (lane & 3) * 4 + (lane >> 5) * 16384;
#pragma unroll
  for (int i = 0; i < 5; ++i) *(unsigned*)(op + i * 32768) = cvtpk((bflo(v[i]) - mean) * r1, (bfhi(v[i]) - mean) * r1);
  if (lane == 0) ((float*)(ws + WS_R2))[row] = __builtin_amdgcn_rsqf(var * r1 * r1 + EPS);
}

constexpr int K1_XB = 16384, K1_QB = 16384, K1_MB = 12288, K1_STG = K1_XB + K1_QB + K1_MB;
constexpr int K1_B1 = 3 * K1_STG, K1_R2 = K1_B1 + 256;
constexpr int K1_LDS = K1_R2 + 512;
#define DIAGK 0
template <int ROLE> struct K1Frag { bf16x8 x; bf16x8 w[ROLE == 2 ? 3 : 4]; };

template <int ROLE>
__device__ __forceinline__ void k1_loop(f32x16 (&acc)[4], char* lds, unsigned lds0, int w, int lane, unsigned vq, unsigned vm0, unsigned vm1,
                                        const char* xg, const char* wq, const char* wm, int rot, char* ws) {
  constexpr int NT = ROLE == 2 ? 3 : 4;
  constexpr int PER = ROLE == 2 ? 5 : 6;
  constexpr int WP = ROLE == 2 ? 192 : 256;
  const int r = lane & 31, hh = lane >> 5, tt = w & 3, q4 = (lane & 15) >> 2;
  const unsigned vx = w * 2048 + lane * 16;
  int ksn = rot;
  auto issue = [&](int slot) {
    const char* xb = xg + ksn * 16384;
    const char* qb = wq + ksn * (64 * 3840);
    const char* qb2 = qb + 32 * 3840;
    const char* mb = wm + ksn * (64 * 3072);
    const unsigned sb = lds0 + slot + w * 1024;
    unsigned sv;
    asm volatile("s_mov_b32 %0, m0\n\t"
                 "s_add_u32 m0, %1, %9\n\ts_nop 0\n\tglobal_load_lds_dwordx4 %2, %5\n\tglobal_load_lds_dwordx4 %2, %5 offset:1024\n\t"
                 "s_add_u32 m0, %1, 0x4000\n\ts_nop 0\n\tglobal_load_lds_dwordx4 %3, %6\n\t"
                 "s_add_u32 m0, %1, 0x6000\n\ts_nop 0\n\tglobal_load_lds_dwordx4 %3, %7\n\t"
                 "s_add_u32 m0, %1, 0x8000\n\ts_nop 0\n\tglobal_load_lds_dwordx4 %4, %8\n\t"
                 "s_mov_b32 m0, %0"
                 : "=&s"(sv) : "s"(sb), "v"(vx), "v"(vq), "v"(vm0), "s"(xb), "s"(qb), "s"(qb2), "s"(mb), "s"(w * 1024) : "memory", "scc");
    if (ROLE != 2) glds16(vm1, mb, sb + 0xA000);
    ksn = ksn == 9 ? 0 : ksn + 1;
  };
  issue(0); issue(K1_STG); issue(2 * K1_STG);
  const int sw = (r >> 1) & 7;
  lds_cptr xp[4], wp[NT];
#pragma unroll
  for (int ks = 0; ks < 4; ++ks) xp[ks] = (lds_cptr)lds + (tt * 32 + r) * 128 + (((2 * ks + hh) ^ sw) << 4);
#pragma unroll
  for (int t = 0; t < NT; ++t)
    wp[t] = (lds_cptr)lds + K1_XB + (ROLE == 2 ? K1_QB : 0) + (8 * hh + q4) * WP + (ROLE == 2 ? t : (t ^ q4)) * 64 + ((lane >> 4) & 1) * 32 + (lane & 3) * 8;
  auto step = [&](K1Frag<ROLE>& fn, const K1Frag<ROLE>& fc, int so, int ks) {
    fn.x = lds_b128(xp[ks] + so);
#pragma unroll
    for (int t = 0; t < NT; ++t) {
      SB();
      if (ROLE == 1) acc[t] = MFMA32(fc.x, fc.w[t], acc[t]);
      else acc[t] = MFMA32(fc.w[t], fc.x, acc[t]);
      SB();
      fn.w[t] = cat8(lds_tr(wp[t] + so + ks * 16 * WP), lds_tr(wp[t] + so + ks * 16 * WP + 4 * WP));
    }
    SB();
  };
  K1Frag<ROLE> f0, f1;
  wait_vm_bar<2 * PER>();
  STAMP(4);
  f0.x = lds_b128(xp[0]);
#pragma unroll
  for (int t = 0; t < NT; ++t) f0.w[t] = cat8(lds_tr(wp[t]), lds_tr(wp[t] + 4 * WP));
  int so = 0;
#pragma nounroll
  for (int s = 0; s < 10; ++s) {
    step(f1, f0, so, 1);
    step(f0, f1, so, 2);
    step(f1, f0, so, 3);
    if (s < 8) wait_vm_bar<PER>(); else wait_vm_bar<0>();
    if (s < 7) issue(so);
    so = so == 2 * K1_STG ? 0 : so + K1_STG;
    step(f0, f1, so, 0);
  }
}

__global__ __launch_bounds__(512, 2) void k1_kernel(const u16* __restrict__ wqkv, const u16* __restrict__ w1, const u16* __restrict__ b1,
                                                     char* __restrict__ ws) {
  __shared__ __attribute__((aligned(16))) char lds[K1_LDS];
  const unsigned lds0 = (unsigned)(uintptr_t)(LDS_AS char*)lds;
  const int b = blockIdx.x, xcd = b & 7, i = b >> 3;
  const int p = (xcd & 1) * 8 + (i & 7), cb = (xcd >> 1) * 4 + (i >> 3);
  const int rot = ((i & 7) + ((i >> 3) * 5 + 1) / 2) % 10;
  const int tid = threadIdx.x, lane = tid & 63, w = __builtin_amdgcn_readfirstlane(tid >> 6);
  const int r = lane & 31, hh = lane >> 5, tt = w & 3;
  STAMP(0);
  const int qt0 = cb < 12 ? cb * 4 : 48 + (cb - 12) * 3;
  const int nq = cb < 12 ? 4 : 3;
  const int qr = 4 * w + (lane >> 4);
  const unsigned vq = (unsigned)(qr * 3840 + (qt0 + min((((lane >> 2) & 3) ^ (qr & 3)), nq - 1)) * 64 + (lane & 3) * 16);
  const int mb0 = w * 64 + lane, mb1 = mb0 + 512;
  const unsigned vm0 = (unsigned)((mb0 / 12) * 3072 + cb * 192 + (mb0 % 12) * 16);
  const unsigned vm1 = (unsigned)((min(mb1, 767) / 12) * 3072 + cb * 192 + (mb1 % 12) * 16);
  if (w == 7) glds4((unsigned)(cb * 192 + min(lane, 47) * 4), b1, lds0 + K1_B1);
  if (w == 5 || w == 6) glds4((unsigned)(p * 512 + (w - 5) * 256 + lane * 4), ws + WS_R2, lds0 + K1_R2 + (w - 5) * 256);
  const char* xg = ws + WS_XN + (size_t)p * 163840;
  const int blk = p * 4 + tt;
  f32x16 acc[4] = {};
  if (w >= 4) {
    k1_loop<2>(acc, lds, lds0, w, lane, vq, vm0, vm1, xg, (const char*)wqkv, (const char*)w1, rot, ws);
    STAMP(1);
    const float rs = *(const float*)(lds + K1_R2 + (tt * 32 + r) * 4);
    char* hp = ws + WS_H + ((size_t)blk * 96 + cb * 6) * 1024 + lane * 16;
#pragma unroll
    for (int t = 0; t < 3; ++t) {
      unsigned pk[8];
#pragma unroll
      for (int g = 0; g < 4; ++g) {
        const u32x2 bb = *(const u32x2*)(lds + K1_B1 + (t * 32 + 8 * g + 4 * hh) * 2);
        const float bv[4] = {bflo(bb[0]), bfhi(bb[0]), bflo(bb[1]), bfhi(bb[1])};
        float v[4];
#pragma unroll
        for (int q = 0; q < 4; ++q) v[q] = gelu_tanh(fmaf(acc[t][4 * g + q], rs, bv[q]));
        pk[2 * g] = cvtpk(v[0], v[1]); pk[2 * g + 1] = cvtpk(v[2], v[3]);
      }
      *(u32x4*)(hp + (t * 2) * 1024) = (u32x4){pk[0], pk[1], pk[2], pk[3]};
      *(u32x4*)(hp + (t * 2 + 1) * 1024) = (u32x4){pk[4], pk[5], pk[6], pk[7]};
    }
  } else if (cb >= 10) {
    k1_loop<1>(acc, lds, lds0, w, lane, vq, vm0, vm1, xg, (const char*)wqkv, (const char*)w1, rot, ws);
    STAMP(1);
#pragma unroll
    for (int t = 0; t < 4; ++t) {
      if (t < nq) {
        const int n = (qt0 + t) * 32 + r - 1280;
        const int head = n / 20, d = n - head * 20;
        char* vp = ws + WS_KV + (size_t)head * 196608 + blk * 3072 + 1536 + (hh * 24 + d) * 16;
        *(u32x4*)(vp) = (u32x4){cvtpk(acc[t][0], acc[t][1]), cvtpk(acc[t][2], acc[t][3]), cvtpk(acc[t][4], acc[t][5]), cvtpk(acc[t][6], acc[t][7])};
        *(u32x4*)(vp + 768) = (u32x4){cvtpk(acc[t][8], acc[t][9]), cvtpk(acc[t][10], acc[t][11]), cvtpk(acc[t][12], acc[t][13]), cvtpk(acc[t][14], acc[t][15])};
        if (d >= 16) {
          const unsigned ov = d == 16 ? 0x3f803f80u : 0u;
          const u32x4 ones = {ov, ov, ov, ov};
          *(u32x4*)(vp + 64) = ones;
          *(u32x4*)(vp + 768 + 64) = ones;
        }
      }
    }
  } else {
    k1_loop<0>(acc, lds, lds0, w, lane, vq, vm0, vm1, xg, (const char*)wqkv, (const char*)w1, rot, ws);
    STAMP(1);
    const bool isq = cb < 5;
    const float rs = isq ? QSCALE : 1.0f;
    char* base = isq ? ws + WS_Q + (size_t)(blk * 32 + r) * 40 : ws + WS_KV + blk * 3072 + r * 16;
    const unsigned hs = isq ? 2048u * 40u : 196608u;
    const unsigned gs = isq ? 16u : 512u;
#pragma unroll
    for (int t = 0; t < 4; ++t) {
#pragma unroll
      for (int g = 0; g < 4; ++g) {
        const u32x2 pk = {cvtpk(acc[t][4 * g] * rs, acc[t][4 * g + 1] * rs), cvtpk(acc[t][4 * g + 2] * rs, acc[t][4 * g + 3] * rs)};
        const unsigned G = (qt0 + t) * 8 + 2 * g + hh - (isq ? 0 : 160);
        const unsigned head = G / 5u, gd = G - head * 5u;
        char* kp = base + (head * hs + (gd >> 1) * gs + (gd & 1) * 8);
        *(u32x2*)kp = pk;
        if (!isq && gd == 4) *(u32x2*)(kp + 8) = (u32x2){0x00003f80u, 0u};
      }
    }
  }
  STAMP(3);
}
#undef DIAGK

constexpr int K2_STG = 24576;
constexpr int K2_MLP = 3 * K2_STG;
constexpr int K2_SLOT = 6144;
constexpr int K2_RP = 164;
constexpr int K2_LDS = K2_MLP + 4 * 32 * K2_RP * 4;

template <int T>
__device__ __forceinline__ void attn_step(f32x16& Sc, f32x16& Sn, f32x16& o, const bf16x8& q0, const bf16x8& q1, bf16x8& k0, bf16x8& k1,
                                          bf16x8& v0, bf16x8& v1, unsigned (&p)[8], float (&e)[4], lds_cptr kc, lds_cptr kn, lds_cptr vc,
                                          lds_cptr vn, int k1off) {
  const f32x16 zero = {};
  const lds_cptr k2p = (T + 2 < 8) ? kc + (T + 2) * 3072 : kn + (T + 2 - 8) * 3072;
  const lds_cptr v1p = (T + 1 < 8) ? vc + (T + 1) * 3072 : vn;
  float a0, a1, a2, a3;
  Sn = MFMA32(k0, q0, zero);
  k0 = lds_b128(k2p);
  SB();
  p[6] = cvtpk(e[0], e[1]); p[7] = cvtpk(e[2], e[3]);
  SB();
  a0 = EXP2(Sc[0]); a1 = EXP2(Sc[1]); a2 = EXP2(Sc[2]); a3 = EXP2(Sc[3]);
  SB();
  o = MFMA32(v1, PK4(p[4], p[5], p[6], p[7]), o);
  v1 = lds_b128(vc + T * 3072 + 768);
  SB();
  p[0] = cvtpk(a0, a1); p[1] = cvtpk(a2, a3);
  SB();
  a0 = EXP2(Sc[4]); a1 = EXP2(Sc[5]); a2 = EXP2(Sc[6]); a3 = EXP2(Sc[7]);
  SB();
  Sn = MFMA32(k1, q1, Sn);
  k1 = lds_b128(k2p + k1off);
  SB();
  p[2] = cvtpk(a0, a1); p[3] = cvtpk(a2, a3);
  SB();
  a0 = EXP2(Sc[8]); a1 = EXP2(Sc[9]); a2 = EXP2(Sc[10]); a3 = EXP2(Sc[11]);
  SB();
  o = MFMA32(v0, PK4(p[0], p[1], p[2], p[3]), o);
  v0 = lds_b128(v1p);
  SB();
  p[4] = cvtpk(a0, a1); p[5] = cvtpk(a2, a3);
  SB();
  e[0] = EXP2(Sc[12]); e[1] = EXP2(Sc[13]); e[2] = EXP2(Sc[14]); e[3] = EXP2(Sc[15]);
  SB();
}

#define DIAGK 1
__global__ __launch_bounds__(768, 3) void k2_kernel(const u16* __restrict__ x, const u16* __restrict__ wo, const u16* __restrict__ w2,
                                                     const u16* __restrict__ b2, char* __restrict__ ws, u16* __restrict__ out) {
  __shared__ __attribute__((aligned(16))) char lds[K2_LDS];
  const unsigned lds0 = (unsigned)(uintptr_t)(LDS_AS char*)lds;
  const int b = blockIdx.x, xcd = b & 7, i = b >> 3;
  const int tid = threadIdx.x, lane = tid & 63, w = __builtin_amdgcn_readfirstlane(tid >> 6);
  const int r = lane & 31, hh = lane >> 5;
  STAMP(0);
  if (w >= 8) {
    const int j = w - 8;
    const int blk = xcd * 8 + (i & 7), cq = i >> 3;
    const char* hg = ws + WS_H + ((size_t)blk * 96 + j * 24) * 1024;
    const char* wg = (const char*)w2 + (size_t)j * 24 * 16 * 1280 + cq * 320;
    const unsigned va = lane * 16;
    unsigned vw[5];
#pragma unroll
    for (int q = 0; q < 5; ++q) { const int u = q * 64 + lane; vw[q] = (unsigned)((u / 20) * 1280 + (u % 20) * 16); }
    const unsigned myl = lds0 + K2_MLP + j * 3 * K2_SLOT;
    auto issue = [&](int ks, int slot) {
      const char* hb = hg + ks * 1024;
      const char* wb = wg + ks * (16 * 1280);
      unsigned sv;
      asm volatile("s_mov_b32 %0, m0\n\t"
                   "s_mov_b32 m0, %1\n\ts_nop 0\n\tglobal_load_lds_dwordx4 %2, %8\n\t"
                   "s_add_u32 m0, %1, 0x400\n\ts_nop 0\n\tglobal_load_lds_dwordx4 %3, %9\n\t"
                   "s_add_u32 m0, %1, 0x800\n\ts_nop 0\n\tglobal_load_lds_dwordx4 %4, %9\n\t"
                   "s_add_u32 m0, %1, 0xc00\n\ts_nop 0\n\tglobal_load_lds_dwordx4 %5, %9\n\t"
                   "s_add_u32 m0, %1, 0x1000\n\ts_nop 0\n\tglobal_load_lds_dwordx4 %6, %9\n\t"
                   "s_add_u32 m0, %1, 0x1400\n\ts_nop 0\n\tglobal_load_lds_dwordx4 %7, %9\n\t"
                   "s_mov_b32 m0, %0"
                   : "=&s"(sv) : "s"(myl + slot), "v"(va), "v"(vw[0]), "v"(vw[1]), "v"(vw[2]), "v"(vw[3]), "v"(vw[4]), "s"(hb), "s"(wb)
                   : "memory", "scc");
    };
    issue(0, 0); issue(1, K2_SLOT); issue(2, 2 * K2_SLOT);
    const lds_cptr ap = (lds_cptr)lds + K2_MLP + j * 3 * K2_SLOT + lane * 16;
    const lds_cptr wp = (lds_cptr)lds + K2_MLP + j * 3 * K2_SLOT + 1024 + (4 * hh + ((lane & 15) >> 2)) * 320 + ((lane >> 4) & 1) * 32 + (lane & 3) * 8;
    f32x16 acc[5] = {};
    bar_raw();
    int so = 0, nb = 0;
#pragma nounroll
    for (int ks = 0; ks < 24; ++ks) {
      if (ks == nb) { bar_raw(); nb += 5; }
      if (ks < 22) wait_vm<12>(); else wait_vm<0>();
      const bf16x8 af = lds_b128(ap + so);
      bf16x8 wf[5];
#pragma unroll
      for (int t = 0; t < 5; ++t) wf[t] = cat8(lds_tr(wp + so + t * 64), lds_tr(wp + so + t * 64 + 8 * 320));
      asm volatile("s_waitcnt lgkmcnt(0)" ::: "memory");
      SB();
      if (ks + 3 < 24) issue(ks + 3, so);
#pragma unroll
      for (int t = 0; t < 5; ++t) acc[t] = MFMA32(wf[t], af, acc[t]);
      so = so == 2 * K2_SLOT ? 0 : so + K2_SLOT;
    }
    STAMP(1);
    bar_raw();
    float* red = (float*)(lds + K2_MLP);
#pragma unroll
    for (int t = 0; t < 5; ++t)
#pragma unroll
      for (int g = 0; g < 4; ++g)
        *(f32x4*)(red + (j * 32 + r) * K2_RP + t * 32 + 8 * g + 4 * hh) = (f32x4){acc[t][4 * g], acc[t][4 * g + 1], acc[t][4 * g + 2], acc[t][4 * g + 3]};
    bar_lds();
#pragma nounroll
    for (int it = lane; it < 160; it += 64) {
      const int tok = j * 8 + it / 20, c8 = it % 20;
      const size_t go = (size_t)(blk * 32 + tok) * 640 + cq * 160 + c8 * 8;
      const u32x4 xv = *(const u32x4*)(x + go);
      const u32x4 bv = *(const u32x4*)(b2 + cq * 160 + c8 * 8);
      f32x4 a0 = {}, a1 = {};
#pragma unroll
      for (int q = 0; q < 4; ++q) {
        const float* pp = red + (q * 32 + tok) * K2_RP + c8 * 8;
        a0 += *(const f32x4*)pp; a1 += *(const f32x4*)(pp + 4);
      }
      *(u32x4*)(out + go) = (u32x4){cvtpk(a0[0] + bflo(xv[0]) + bflo(bv[0]), a0[1] + bfhi(xv[0]) + bfhi(bv[0])),
                                    cvtpk(a0[2] + bflo(xv[1]) + bflo(bv[1]), a0[3] + bfhi(xv[1]) + bfhi(bv[1])),
                                    cvtpk(a1[0] + bflo(xv[2]) + bflo(bv[2]), a1[1] + bfhi(xv[2]) + bfhi(bv[2])),
                                    cvtpk(a1[2] + bflo(xv[3]) + bflo(bv[3]), a1[3] + bfhi(xv[3]) + bfhi(bv[3]))};
    }
    const int f = b * 4 + j;
    if (f < 800) {
      const int t = f % 5, ks = (f / 5) % 40, c2 = f / 200;
      const u16* wp2 = wo + (size_t)(ks * 16 + 4 * hh) * 640 + c2 * 160 + t * 32 + r;
      unsigned e[8];
#pragma unroll
      for (int q = 0; q < 8; ++q) e[q] = wp2[(8 * (q >> 2) + (q & 3)) * 640];
      *(u32x4*)(ws + WS_WOT + (size_t)f * 1024 + lane * 16) = (u32x4){e[0] | (e[1] << 16), e[2] | (e[3] << 16), e[4] | (e[5] << 16), e[6] | (e[7] << 16)};
    }
    STAMP(3);
    return;
  }
  const int head = xcd * 4 + (i & 3), qb = i >> 2;
  const char* kvg = ws + WS_KV + (size_t)head * 196608;
  const unsigned vdma = (unsigned)(w * 3072 + lane * 16);
  glds16x3(vdma, kvg, lds0 + w * 3072);
  const int token = qb * 256 + w * 32 + r;
  const char* qp = ws + WS_Q + ((size_t)head * 2048 + token) * 40;
  const u32x2 qa = *(const u32x2*)(qp + hh * 16), qb2 = *(const u32x2*)(qp + hh * 16 + 8), qc = *(const u32x2*)(qp + 32);
  u32x4 q0u = {qa[0], qa[1], qb2[0], qb2[1]};
  u32x4 q1u = {hh ? 0u : qc[0], hh ? 0u : qc[1], 0u, 0u};
  asm volatile("" : "+v"(q0u), "+v"(q1u));
  glds16x3(vdma, kvg + K2_STG, lds0 + K2_STG + w * 3072);
  const bf16x8 q0 = __builtin_bit_cast(bf16x8, q0u);
  const int dcl = r < 21 ? r : 21;
  const int k1off = hh ? 1536 + 21 * 16 - lane * 16 : 1024;
  const lds_cptr kb = (lds_cptr)lds + lane * 16;
  const lds_cptr vb = (lds_cptr)lds + 1536 + (hh * 24 + dcl) * 16;
  const f32x16 zero = {};
  wait_vm_bar<3>();
  bf16x8 k0 = lds_b128(kb), k1 = lds_b128(kb + k1off);
  f32x16 sa = MFMA32(k0, q0, zero), sb;
  {
    f32x16 s = MFMA32(k1, __builtin_bit_cast(bf16x8, q1u), sa);
    float m = s[0];
#pragma unroll
    for (int q = 1; q < 16; ++q) m = fmaxf(m, s[q]);
    m = xhalf_max(m);
    q1u[2] = hh ? 0u : (cvtpk(-m, 0.0f) & 0xffffu);
  }
  const bf16x8 q1 = __builtin_bit_cast(bf16x8, q1u);
  sa = MFMA32(k1, q1, sa);
  k0 = lds_b128(kb + 3072); k1 = lds_b128(kb + 3072 + k1off);
  bf16x8 v0 = lds_b128(vb), v1 = {};
  unsigned p[8] = {};
  float e[4] = {};
  f32x16 o = {};
  int sc = 0;
  STAMP(1);
#pragma nounroll
  for (int st = 0; st < 8; ++st) {
    const int sn = sc == 2 * K2_STG ? 0 : sc + K2_STG;
    const lds_cptr kc = kb + sc, kn = kb + sn, vc = vb + sc, vn = vb + sn;
    attn_step<0>(sa, sb, o, q0, q1, k0, k1, v0, v1, p, e, kc, kn, vc, vn, k1off);
    attn_step<1>(sb, sa, o, q0, q1, k0, k1, v0, v1, p, e, kc, kn, vc, vn, k1off);
    attn_step<2>(sa, sb, o, q0, q1, k0, k1, v0, v1, p, e, kc, kn, vc, vn, k1off);
    attn_step<3>(sb, sa, o, q0, q1, k0, k1, v0, v1, p, e, kc, kn, vc, vn, k1off);
    asm volatile("s_waitcnt vmcnt(0)\n\ts_barrier" ::: "memory");
    if (st + 2 < 8) {
      const int sp = sc == 0 ? 2 * K2_STG : sc - K2_STG;
      glds16x3(vdma, kvg + (st + 2) * K2_STG, lds0 + sp + w * 3072);
    }
    SB();
    attn_step<4>(sa, sb, o, q0, q1, k0, k1, v0, v1, p, e, kc, kn, vc, vn, k1off);
    attn_step<5>(sb, sa, o, q0, q1, k0, k1, v0, v1, p, e, kc, kn, vc, vn, k1off);
    attn_step<6>(sa, sb, o, q0, q1, k0, k1, v0, v1, p, e, kc, kn, vc, vn, k1off);
    attn_step<7>(sb, sa, o, q0, q1, k0, k1, v0, v1, p, e, kc, kn, vc, vn, k1off);
    sc = sn;
  }
  STAMP(2);
  p[6] = cvtpk(e[0], e[1]); p[7] = cvtpk(e[2], e[3]);
  o = MFMA32(v1, PK4(p[4], p[5], p[6], p[7]), o);
  float l = upper_half(o[8]);
  float amax = 0.f;
#pragma unroll
  for (int q = 0; q < 12; ++q) amax = fmaxf(amax, fabsf(o[q]));
  const bool bad = !(l < 1.0e9f) || !(l > 0.25f) || !(amax < 1.0e30f);
  if (__builtin_expect(__any(bad), 0)) {
    q1u[2] = 0u;
    const bf16x8 q1z = __builtin_bit_cast(bf16x8, q1u);
    float m = -INFINITY;
    o = zero;
#pragma nounroll
    for (int t = 0; t < 64; ++t) {
      const char* tp = kvg + t * 3072;
      f32x16 s = MFMA32(*(const bf16x8*)(tp + lane * 16), q0, zero);
      s = MFMA32(*(const bf16x8*)(tp + k1off + lane * 16), q1z, s);
      float tm = s[0];
#pragma unroll
      for (int q = 1; q < 16; ++q) tm = fmaxf(tm, s[q]);
      tm = xhalf_max(tm);
      const float mn = fmaxf(m, tm);
      const float al = EXP2(m - mn);
      m = mn;
      unsigned pk[8];
#pragma unroll
      for (int q = 0; q < 8; ++q) pk[q] = cvtpk(EXP2(s[2 * q] - mn), EXP2(s[2 * q + 1] - mn));
#pragma unroll
      for (int q = 0; q < 16; ++q) o[q] *= al;
      const char* vp = tp + 1536 + (hh * 24 + dcl) * 16;
      o = MFMA32(*(const bf16x8*)(vp), PK4(pk[0], pk[1], pk[2], pk[3]), o);
      o = MFMA32(*(const bf16x8*)(vp + 768), PK4(pk[4], pk[5], pk[6], pk[7]), o);
    }
    l = upper_half(o[8]);
  }
  const float il = 1.0f / l;
  char* op = ws + WS_O + (size_t)(qb * 8 + w) * 40 * 1024;
#pragma unroll
  for (int g = 0; g < 3; ++g) {
    if (g < 2 || hh == 0) {
      const int G = head * 5 + 2 * g + hh;
      const u32x2 pk = {cvtpk(o[4 * g] * il, o[4 * g + 1] * il), cvtpk(o[4 * g + 2] * il, o[4 * g + 3] * il)};
      *(u32x2*)(op + (G >> 2) * 1024 + ((G & 1) * 32 + r) * 16 + ((G >> 1) & 1) * 8) = pk;
    }
  }
  STAMP(3);
}
#undef DIAGK

#define DIAGK 2
__global__ __launch_bounds__(512, 2) void k3_kernel(char* __restrict__ ws, u16* __restrict__ out) {
  constexpr int RP = 164;
  __shared__ __attribute__((aligned(16))) float red[4 * 32 * RP];
  const int b = blockIdx.x, xcd = b & 7, i = b >> 3;
  const int blk = xcd * 8 + (i & 7), cq = i >> 3;
  const int tid = threadIdx.x, lane = tid & 63, w = __builtin_amdgcn_readfirstlane(tid >> 6);
  const int r = lane & 31, hh = lane >> 5;
  STAMP(0);
  const char* ap = ws + WS_O + ((size_t)blk * 40 + w * 5) * 1024 + lane * 16;
  const char* wp = ws + WS_WOT + ((size_t)cq * 40 + w * 5) * 5 * 1024 + lane * 16;
  bf16x8 af[5], wf[5][5];
#pragma unroll
  for (int k = 0; k < 5; ++k) {
    af[k] = *(const bf16x8*)(ap + k * 1024);
#pragma unroll
    for (int t = 0; t < 5; ++t) wf[k][t] = *(const bf16x8*)(wp + (k * 5 + t) * 1024);
  }
  u32x4 pv[2];
  const int it1 = tid + 512;
  const size_t go0 = (size_t)(blk * 32 + tid / 20) * 640 + cq * 160 + (tid % 20) * 8;
  const size_t go1 = (size_t)(blk * 32 + min(it1, 639) / 20) * 640 + cq * 160 + (it1 % 20) * 8;
  pv[0] = *(const u32x4*)(out + go0);
  pv[1] = *(const u32x4*)(out + go1);
  SB();
  f32x16 acc[5] = {};
#pragma unroll
  for (int k = 0; k < 5; ++k)
#pragma unroll
    for (int t = 0; t < 5; ++t) acc[t] = MFMA32(wf[k][t], af[k], acc[t]);
  STAMP(1);
  if (w >= 4) {
#pragma unroll
    for (int t = 0; t < 5; ++t)
#pragma unroll
      for (int g = 0; g < 4; ++g)
        *(f32x4*)(red + ((w - 4) * 32 + r) * RP + t * 32 + 8 * g + 4 * hh) = (f32x4){acc[t][4 * g], acc[t][4 * g + 1], acc[t][4 * g + 2], acc[t][4 * g + 3]};
  }
  bar_lds();
  if (w < 4) {
#pragma unroll
    for (int t = 0; t < 5; ++t)
#pragma unroll
      for (int g = 0; g < 4; ++g) {
        float* pp = red + (w * 32 + r) * RP + t * 32 + 8 * g + 4 * hh;
        f32x4 v = *(f32x4*)pp;
        v += (f32x4){acc[t][4 * g], acc[t][4 * g + 1], acc[t][4 * g + 2], acc[t][4 * g + 3]};
        *(f32x4*)pp = v;
      }
  }
  bar_lds();
  STAMP(2);
#pragma unroll
  for (int u = 0; u < 2; ++u) {
    const int it = tid + u * 512;
    if (it < 640) {
      const int tok = it / 20, c8 = it % 20;
      f32x4 a0 = {}, a1 = {};
#pragma unroll
      for (int q = 0; q < 4; ++q) {
        const float* pp = red + (q * 32 + tok) * RP + c8 * 8;
        a0 += *(const f32x4*)pp; a1 += *(const f32x4*)(pp + 4);
      }
      const u32x4 xv = pv[u];
      *(u32x4*)(out + (u ? go1 : go0)) = (u32x4){cvtpk(a0[0] + bflo(xv[0]), a0[1] + bfhi(xv[0])), cvtpk(a0[2] + bflo(xv[1]), a0[3] + bfhi(xv[1])),
                                                 cvtpk(a1[0] + bflo(xv[2]), a1[1] + bfhi(xv[2])), cvtpk(a1[2] + bflo(xv[3]), a1[3] + bfhi(xv[3]))};
    }
  }
  STAMP(3);
}
#undef DIAGK

extern "C" void kernel_launch(void* const* d_in, const int* in_sizes, int n_in, void* d_out, int out_size, void* d_ws, size_t ws_size,
                              hipStream_t stream) {
  const u16* x = (const u16*)d_in[0];
  const u16* wqkv = (const u16*)d_in[1];
  const u16* wo = (const u16*)d_in[2];
  const u16* w1 = (const u16*)d_in[4];
  const u16* b1 = (const u16*)d_in[5];
  const u16* w2 = (const u16*)d_in[6];
  const u16* b2 = (const u16*)d_in[7];
  char* ws = (char*)d_ws;
  k0_kernel<<<256, 512, 0, stream>>>(x, ws);
  k1_kernel<<<256, 512, 0, stream>>>(wqkv, w1, b1, ws);
  k2_kernel<<<256, 768, 0, stream>>>(x, wo, w2, b2, ws, (u16*)d_out);
  k3_kernel<<<256, 512, 0, stream>>>(ws, (u16*)d_out);
}
```

```cpp
#include <hip/hip_runtime.h>
#include <hip/hip_bf16.h>
#include <stdint.h>

typedef unsigned short u16;
typedef unsigned long long u64;
typedef short bf16x8 __attribute__((ext_vector_type(8)));
typedef short s16x4 __attribute__((ext_vector_type(4)));
typedef float f32x16 __attribute__((ext_vector_type(16)));
typedef float f32x4 __attribute__((ext_vector_type(4)));
typedef float f32x2 __attribute__((ext_vector_type(2)));
typedef unsigned u32x4 __attribute__((ext_vector_type(4)));
typedef unsigned u32x2 __attribute__((ext_vector_type(2)));
typedef __bf16 bf2 __attribute__((ext_vector_type(2)));
#define LDS_AS __attribute__((address_space(3)))
#define GAS __attribute__((address_space(1)))
typedef LDS_AS const char* lds_cptr;
typedef GAS u64 gu64;
#define RLX_AGENT __ATOMIC_RELAXED, __HIP_MEMORY_SCOPE_AGENT

constexpr float EPS = 1e-5f;
constexpr float QSCALE = 0.22360679774997896f * 1.4426950408889634f;

constexpr unsigned WS_SYNC = 0;
constexpr unsigned WS_F0 = 4096, WS_F1 = WS_F0 + 32768, WS_F1B = WS_F1 + 32768, WS_F2 = WS_F1B + 32768;
constexpr unsigned WS_Q = WS_F2 + 32768;
constexpr unsigned WS_KV = WS_Q + 32 * 64 * 1536;
constexpr unsigned WS_O = WS_KV + 32 * 196608;
constexpr unsigned WS_H = WS_O + 64 * 40 * 1024;
constexpr unsigned WS_XN = WS_H + 64 * 96 * 1024;
constexpr unsigned WS_R2 = WS_XN + 2048 * 1280;
constexpr unsigned WS_WT = WS_R2 + 8192;
constexpr unsigned WS_END = WS_WT + 8 * 68 * 5 * 1024;

#define MFMA32(a, b, c) __builtin_amdgcn_mfma_f32_32x32x16_bf16(a, b, c, 0, 0, 0)
#define MFMA16(a, b, c) __builtin_amdgcn_mfma_f32_16x16x32_bf16(a, b, c, 0, 0, 0)
#define SB() __builtin_amdgcn_sched_barrier(0)
#define EXP2(v) __builtin_amdgcn_exp2f(v)
#define PK4(a, b, c, d) __builtin_bit_cast(bf16x8, ((u32x4){a, b, c, d}))
#define ST16(v, off) __builtin_amdgcn_raw_buffer_store_b128(v, wsr, off, 0, 16)
#define ST8(v, off) __builtin_amdgcn_raw_buffer_store_b64(v, wsr, off, 0, 16)
#define LD16(off, soff) __builtin_bit_cast(bf16x8, __builtin_amdgcn_raw_buffer_load_b128(wsr, off, soff, 16))

#define STAMP(i)

__device__ __forceinline__ unsigned cvtpk(float lo, float hi) {
  return __builtin_bit_cast(unsigned, __builtin_convertvector((f32x2){lo, hi}, bf2));
}
__device__ __forceinline__ float bflo(unsigned u) { return __uint_as_float(u << 16); }
__device__ __forceinline__ float bfhi(unsigned u) { return __uint_as_float(u & 0xffff0000u); }
__device__ __forceinline__ void glds4(unsigned voff, const void* sbase, unsigned ldsb) {
  unsigned sv;
  asm volatile("s_mov_b32 %0, m0\n\ts_mov_b32 m0, %3\n\ts_nop 0\n\tglobal_load_lds_dword %1, %2 sc1\n\ts_mov_b32 m0, %0"
               : "=&s"(sv) : "v"(voff), "s"(sbase), "s"(ldsb) : "memory");
}
__device__ __forceinline__ void glds16x3(unsigned voff, const void* sbase, unsigned ldsb) {
  unsigned sv;
  asm volatile("s_mov_b32 %0, m0\n\ts_mov_b32 m0, %3\n\ts_nop 0\n\t"
               "global_load_lds_dwordx4 %1, %2 sc1\n\t"
               "global_load_lds_dwordx4 %1, %2 offset:1024 sc1\n\t"
               "global_load_lds_dwordx4 %1, %2 offset:2048 sc1\n\t"
               "s_mov_b32 m0, %0"
               : "=&s"(sv) : "v"(voff), "s"(sbase), "s"(ldsb) : "memory");
}
__device__ __forceinline__ void glds16x4(unsigned voff, const void* sbase, unsigned ldsb) {
  unsigned sv;
  asm volatile("s_mov_b32 %0, m0\n\ts_mov_b32 m0, %3\n\ts_nop 0\n\t"
               "global_load_lds_dwordx4 %1, %2 sc1\n\t"
               "global_load_lds_dwordx4 %1, %2 offset:1024 sc1\n\t"
               "global_load_lds_dwordx4 %1, %2 offset:2048 sc1\n\t"
               "global_load_lds_dwordx4 %1, %2 offset:3072 sc1\n\t"
               "s_mov_b32 m0, %0"
               : "=&s"(sv) : "v"(voff), "s"(sbase), "s"(ldsb) : "memory");
}
template <int N> __device__ __forceinline__ void wait_vm() { asm volatile("s_waitcnt vmcnt(%0)" ::"i"(N) : "memory"); }
template <int N> __device__ __forceinline__ void wait_vm_bar() {
  asm volatile("s_waitcnt vmcnt(%0) lgkmcnt(0)\n\ts_barrier" ::"i"(N) : "memory");
}
__device__ __forceinline__ void bar_lds() { asm volatile("s_waitcnt lgkmcnt(0)\n\ts_barrier" ::: "memory"); }
__device__ __forceinline__ bf16x8 lds_b128(lds_cptr p) { return *(const LDS_AS bf16x8*)p; }
__device__ __forceinline__ s16x4 lds_tr(lds_cptr p) { return __builtin_amdgcn_ds_read_tr16_b64_v4i16((LDS_AS s16x4*)p); }
__device__ __forceinline__ bf16x8 cat8(s16x4 a, s16x4 b) { return __builtin_shufflevector(a, b, 0, 1, 2, 3, 4, 5, 6, 7); }
__device__ __forceinline__ float xhalf_max(float v) {
  auto rr = __builtin_amdgcn_permlane32_swap(__float_as_uint(v), __float_as_uint(v), false, false);
  return fmaxf(__uint_as_float(rr[0]), __uint_as_float(rr[1]));
}
__device__ __forceinline__ float upper_half(float v) {
  auto rr = __builtin_amdgcn_permlane32_swap(__float_as_uint(v), __float_as_uint(v), false, false);
  return __uint_as_float(rr[1]);
}
__device__ __forceinline__ float gelu_tanh(float x) {
  const float c0 = -2.0f * 0.7978845608028654f * 1.4426950408889634f;
  const float c1 = c0 * 0.044715f;
  float t = fmaf(x * x, c1, c0);
  float e = __builtin_amdgcn_exp2f(x * t);
  return x * __builtin_amdgcn_rcpf(1.0f + e);
}
template <int CTRL, int RMASK> __device__ __forceinline__ float dpp_add(float v) {
  return v + __uint_as_float(__builtin_amdgcn_update_dpp(0u, __float_as_uint(v), CTRL, RMASK, 0xf, false));
}
__device__ __forceinline__ float wave_sum(float v) {
  v = dpp_add<0x111, 0xf>(v); v = dpp_add<0x112, 0xf>(v); v = dpp_add<0x114, 0xf>(v); v = dpp_add<0x118, 0xf>(v);
  v = dpp_add<0x142, 0xa>(v); v = dpp_add<0x143, 0xc>(v);
  return __uint_as_float(__builtin_amdgcn_readlane(__float_as_uint(v), 63));
}
__device__ __forceinline__ u64 ld_flag(const char* ws, unsigned off) { return __hip_atomic_load((gu64*)(ws + off), RLX_AGENT); }
__device__ __forceinline__ void st_flag(char* ws, unsigned off, u64 v) { __hip_atomic_store((gu64*)(ws + off), v, RLX_AGENT); }
constexpr unsigned SPIN_CAP = 1u << 18;
__device__ __forceinline__ void poll2(const char* ws, unsigned off1, unsigned off2, u64 epoch) {
  for (unsigned s = 0; s < SPIN_CAP; ++s) {
    const u64 a = ld_flag(ws, off1), b = ld_flag(ws, off2);
    if (__all(a == epoch && b == epoch)) break;
    __builtin_amdgcn_s_sleep(1);
  }
  asm volatile("" ::: "memory");
}

constexpr int P1_XB = 16384, P1_QB = 16384, P1_MB = 12288, P1_STG = P1_XB + P1_QB + P1_MB;
constexpr int P1_B1 = 3 * P1_STG, P1_R2 = P1_B1 + 256, P1_CNT = P1_R2 + 512;
constexpr int LDS_BYTES = 163840;

template <int ROLE> struct P1Frag { bf16x8 x; bf16x8 w[ROLE == 2 ? 3 : 4]; };

struct P1Dma {
  unsigned vq, vm[3];
  const char *wq, *wm;
};
__device__ __forceinline__ void p1_issue_w(const P1Dma& d, int ks, unsigned sb  ) {
  const char* qb = d.wq + ks * (64 * 3840);
  const char* qb1 = qb + 16 * 3840;
  const char* qb2 = qb + 32 * 3840;
  const char* qb3 = qb + 48 * 3840;
  const char* mb = d.wm + ks * (64 * 3072);
  unsigned sv;
  asm volatile("s_mov_b32 %0, m0\n\t"
               "s_add_u32 m0, %1, 0x4000\n\ts_nop 0\n\tglobal_load_lds_dwordx4 %2, %6\n\t"
               "s_add_u32 m0, %1, 0x5000\n\ts_nop 0\n\tglobal_load_lds_dwordx4 %2, %7\n\t"
               "s_add_u32 m0, %1, 0x6000\n\ts_nop 0\n\tglobal_load_lds_dwordx4 %2, %8\n\t"
               "s_add_u32 m0, %1, 0x7000\n\ts_nop 0\n\tglobal_load_lds_dwordx4 %2, %9\n\t"
               "s_add_u32 m0, %1, 0x8000\n\ts_nop 0\n\tglobal_load_lds_dwordx4 %3, %10\n\t"
               "s_add_u32 m0, %1, 0x9000\n\ts_nop 0\n\tglobal_load_lds_dwordx4 %4, %10\n\t"
               "s_add_u32 m0, %1, 0xa000\n\ts_nop 0\n\tglobal_load_lds_dwordx4 %5, %10\n\t"
               "s_mov_b32 m0, %0"
               : "=&s"(sv) : "s"(sb), "v"(d.vq), "v"(d.vm[0]), "v"(d.vm[1]), "v"(d.vm[2]), "s"(qb), "s"(qb1), "s"(qb2), "s"(qb3), "s"(mb)
               : "memory", "scc");
}

template <int ROLE>
__device__ __forceinline__ void p1_loop(f32x16 (&acc)[4], char* lds, unsigned lds0, int w, int lane, const P1Dma& d, const char* xg, int rot3) {
  constexpr int NT = ROLE == 2 ? 3 : 4;
  constexpr int PER = ROLE == 2 ? 7 : 4;
  constexpr int WP = ROLE == 2 ? 192 : 256;
  const int r = lane & 31, hh = lane >> 5, tt = w & 3, q4 = (lane & 15) >> 2;
  const unsigned vx = tt * 4096 + lane * 16;
  int ksn = rot3;
  const int sw = (r >> 1) & 7;
  lds_cptr xp[4], wp[NT];
#pragma unroll
  for (int ks = 0; ks < 4; ++ks) xp[ks] = (lds_cptr)lds + (tt * 32 + r) * 128 + (((2 * ks + hh) ^ sw) << 4);
#pragma unroll
  for (int t = 0; t < NT; ++t)
    wp[t] = (lds_cptr)lds + P1_XB + (ROLE == 2 ? P1_QB : 0) + (8 * hh + q4) * WP + (ROLE == 2 ? t : (t ^ q4)) * 64 + ((lane >> 4) & 1) * 32 + (lane & 3) * 8;
  auto step = [&](P1Frag<ROLE>& fn, const P1Frag<ROLE>& fc, int so, int ks) {
    fn.x = lds_b128(xp[ks] + so);
#pragma unroll
    for (int t = 0; t < NT; ++t) {
      SB();
      if (ROLE == 1) acc[t] = MFMA32(fc.x, fc.w[t], acc[t]);
      else acc[t] = MFMA32(fc.w[t], fc.x, acc[t]);
      SB();
      fn.w[t] = cat8(lds_tr(wp[t] + so + ks * 16 * WP), lds_tr(wp[t] + so + ks * 16 * WP + 4 * WP));
    }
    SB();
  };
  P1Frag<ROLE> f0, f1;
  wait_vm_bar<2 * PER>();
  f0.x = lds_b128(xp[0]);
#pragma unroll
  for (int t = 0; t < NT; ++t) f0.w[t] = cat8(lds_tr(wp[t]), lds_tr(wp[t] + 4 * WP));
  int so = 0;
#pragma nounroll
  for (int s = 0; s < 10; ++s) {
    step(f1, f0, so, 1);
    step(f0, f1, so, 2);
    step(f1, f0, so, 3);
    if (s < 8) wait_vm_bar<PER>(); else wait_vm_bar<0>();
    if (s < 7) {
      if (ROLE == 2) p1_issue_w(d, ksn, lds0 + so + tt * 1024);
      else glds16x4(vx, xg + ksn * 16384, lds0 + so + tt * 4096);
      ksn = ksn == 9 ? 0 : ksn + 1;
    }
    so = so == 2 * P1_STG ? 0 : so + P1_STG;
    step(f0, f1, so, 0);
  }
}

constexpr int P2_STG = 24576;
template <int T>
__device__ __forceinline__ void attn_step(f32x16& Sc, f32x16& Sn, f32x16& o, const bf16x8& q0, const bf16x8& q1, bf16x8& k0, bf16x8& k1,
                                          bf16x8& v0, bf16x8& v1, unsigned (&p)[8], float (&e)[4], lds_cptr kc, lds_cptr kn, lds_cptr vc,
                                          lds_cptr vn, int k1off) {
  const f32x16 zero = {};
  const lds_cptr k2p = (T + 2 < 8) ? kc + (T + 2) * 3072 : kn + (T + 2 - 8) * 3072;
  const lds_cptr v1p = (T + 1 < 8) ? vc + (T + 1) * 3072 : vn;
  float a0, a1, a2, a3;
  Sn = MFMA32(k0, q0, zero);
  k0 = lds_b128(k2p);
  SB();
  p[6] = cvtpk(e[0], e[1]); p[7] = cvtpk(e[2], e[3]);
  SB();
  a0 = EXP2(Sc[0]); a1 = EXP2(Sc[1]); a2 = EXP2(Sc[2]); a3 = EXP2(Sc[3]);
  SB();
  o = MFMA32(v1, PK4(p[4], p[5], p[6], p[7]), o);
  v1 = lds_b128(vc + T * 3072 + 768);
  SB();
  p[0] = cvtpk(a0, a1); p[1] = cvtpk(a2, a3);
  SB();
  a0 = EXP2(Sc[4]); a1 = EXP2(Sc[5]); a2 = EXP2(Sc[6]); a3 = EXP2(Sc[7]);
  SB();
  Sn = MFMA32(k1, q1, Sn);
  k1 = lds_b128(k2p + k1off);
  SB();
  p[2] = cvtpk(a0, a1); p[3] = cvtpk(a2, a3);
  SB();
  a0 = EXP2(Sc[8]); a1 = EXP2(Sc[9]); a2 = EXP2(Sc[10]); a3 = EXP2(Sc[11]);
  SB();
  o = MFMA32(v0, PK4(p[0], p[1], p[2], p[3]), o);
  v0 = lds_b128(v1p);
  SB();
  p[4] = cvtpk(a0, a1); p[5] = cvtpk(a2, a3);
  SB();
  e[0] = EXP2(Sc[12]); e[1] = EXP2(Sc[13]); e[2] = EXP2(Sc[14]); e[3] = EXP2(Sc[15]);
  SB();
}

__device__ __forceinline__ int v_colblock(int tile) { return tile < 48 ? tile >> 2 : 12 + (tile - 48) / 3; }

__global__ __launch_bounds__(512, 2) void mega_kernel(const u16* __restrict__ x, const u16* __restrict__ wqkv, const u16* __restrict__ wo,
                                                       const u16* __restrict__ w1, const u16* __restrict__ b1, const u16* __restrict__ w2,
                                                       const u16* __restrict__ b2, char* ws, u16* __restrict__ out) {
  __shared__ __attribute__((aligned(16))) char lds[LDS_BYTES];
  const unsigned lds0 = (unsigned)(uintptr_t)(LDS_AS char*)lds;
  const int b = blockIdx.x, xcd = b & 7, i = b >> 3;
  const int tid = threadIdx.x, lane = tid & 63, w = __builtin_amdgcn_readfirstlane(tid >> 6);
  const int r = lane & 31, hh = lane >> 5, tt = w & 3;
  const auto wsr = __builtin_amdgcn_make_buffer_rsrc(ws, 0, WS_END, 0x00020000);
  STAMP(0);
  u64 epoch = ld_flag(ws, WS_SYNC) + 1;

  {
    const int p = (xcd & 1) * 8 + (i & 7), cb = (xcd >> 1) * 4 + (i >> 3);
    const int id1 = p * 16 + cb;
    const int rot = ((i & 7) + ((i >> 3) * 5 + 1) / 2) % 10;
    const int rot1 = rot == 9 ? 0 : rot + 1, rot2 = rot1 == 9 ? 0 : rot1 + 1, rot3 = rot2 == 9 ? 0 : rot2 + 1;
    const int qt0 = cb < 12 ? cb * 4 : 48 + (cb - 12) * 3;
    const int nq = cb < 12 ? 4 : 3;
    const char* xg = ws + WS_XN + p * 163840;
    const int blk = p * 4 + tt;
    P1Dma d;
    f32x16 acc[4] = {};
    if (w >= 4) {
      const int qr = 4 * tt + (lane >> 4);
      d.vq = (unsigned)(qr * 3840 + (qt0 + min((((lane >> 2) & 3) ^ (qr & 3)), nq - 1)) * 64 + (lane & 3) * 16);
#pragma unroll
      for (int q = 0; q < 3; ++q) { const int u = q * 256 + tt * 64 + lane; d.vm[q] = (unsigned)((u / 12) * 3072 + cb * 192 + (u % 12) * 16); }
      d.wq = (const char*)wqkv; d.wm = (const char*)w1;
      if (w == 7) glds4((unsigned)(cb * 192 + min(lane, 47) * 4), b1, lds0 + P1_B1);
      p1_issue_w(d, rot, lds0 + tt * 1024);
      p1_issue_w(d, rot1, lds0 + P1_STG + tt * 1024);
      p1_issue_w(d, rot2, lds0 + 2 * P1_STG + tt * 1024);
      asm volatile("s_barrier\n\ts_barrier" ::: "memory");
      p1_loop<2>(acc, lds, lds0, w, lane, d, xg, rot3);
      STAMP(3);
      const float rs = *(const float*)(lds + P1_R2 + (tt * 32 + r) * 4);
      const unsigned hp = WS_H + (blk * 96 + cb * 6) * 1024 + lane * 16;
#pragma unroll
      for (int t = 0; t < 3; ++t) {
        unsigned pk[8];
#pragma unroll
        for (int g = 0; g < 4; ++g) {
          const u32x2 bb = *(const u32x2*)(lds + P1_B1 + (t * 32 + 8 * g + 4 * hh) * 2);
          const float bv[4] = {bflo(bb[0]), bfhi(bb[0]), bflo(bb[1]), bfhi(bb[1])};
          float v[4];
#pragma unroll
          for (int q = 0; q < 4; ++q) v[q] = gelu_tanh(fmaf(acc[t][4 * g + q], rs, bv[q]));
          pk[2 * g] = cvtpk(v[0], v[1]); pk[2 * g + 1] = cvtpk(v[2], v[3]);
        }
        ST16(((u32x4){pk[0], pk[1], pk[2], pk[3]}), hp + (t * 2) * 1024);
        ST16(((u32x4){pk[4], pk[5], pk[6], pk[7]}), hp + (t * 2 + 1) * 1024);
      }
    } else {
      if (tid == 0) *(LDS_AS unsigned*)(LDS_AS char*)&lds[P1_CNT] = 0u;
      const int rr0 = cb * 8 + 2 * tt;
      u32x4 xa[2], xb[2];
#pragma unroll
      for (int q = 0; q < 2; ++q) {
        const u16* xr = x + (size_t)(p * 128 + rr0 + q) * 640;
        xa[q] = *(const u32x4*)(xr + lane * 8);
        xb[q] = *(const u32x4*)(xr + 512 + (lane & 15) * 8);
      }
#pragma unroll
      for (int q = 0; q < 2; ++q) {
        const float c = __uint_as_float(__builtin_amdgcn_readfirstlane(xa[q][0]) << 16);
        float va[8], vb[8], s1 = 0.f, s2 = 0.f, t1 = 0.f, t2 = 0.f;
#pragma unroll
        for (int e = 0; e < 4; ++e) {
          va[2 * e] = bflo(xa[q][e]) - c; va[2 * e + 1] = bfhi(xa[q][e]) - c;
          vb[2 * e] = bflo(xb[q][e]) - c; vb[2 * e + 1] = bfhi(xb[q][e]) - c;
        }
#pragma unroll
        for (int e = 0; e < 8; ++e) { s1 += va[e]; s2 = fmaf(va[e], va[e], s2); t1 += vb[e]; t2 = fmaf(vb[e], vb[e], t2); }
        if (lane < 16) { s1 += t1; s2 += t2; }
        const float m = wave_sum(s1) * (1.0f / 640.0f);
        const float var = fmaxf(wave_sum(s2) * (1.0f / 640.0f) - m * m, 0.0f);
        const float r1 = __builtin_amdgcn_rsqf(var + EPS);
        const float nm = -m * r1;
        const int rr = rr0 + q;
        const unsigned o0 = WS_XN + p * 163840 + (lane >> 3) * 16384 + rr * 128 + (((lane & 7) ^ ((rr >> 1) & 7)) << 4);
        ST16(((u32x4){cvtpk(fmaf(va[0], r1, nm), fmaf(va[1], r1, nm)), cvtpk(fmaf(va[2], r1, nm), fmaf(va[3], r1, nm)),
                      cvtpk(fmaf(va[4], r1, nm), fmaf(va[5], r1, nm)), cvtpk(fmaf(va[6], r1, nm), fmaf(va[7], r1, nm))}), o0);
        if (lane < 16) {
          ST16(((u32x4){cvtpk(fmaf(vb[0], r1, nm), fmaf(vb[1], r1, nm)), cvtpk(fmaf(vb[2], r1, nm), fmaf(vb[3], r1, nm)),
                        cvtpk(fmaf(vb[4], r1, nm), fmaf(vb[5], r1, nm)), cvtpk(fmaf(vb[6], r1, nm), fmaf(vb[7], r1, nm))}), o0 + 8 * 16384);
          if (lane == 0)
            __builtin_amdgcn_raw_buffer_store_b32(__float_as_uint(__builtin_amdgcn_rsqf(var * r1 * r1 + EPS)), wsr, WS_R2 + (p * 128 + rr) * 4, 0, 16);
        }
      }
      wait_vm_bar<0>();
      STAMP(1);
      if (w == 0) {
        if (lane == 0) st_flag(ws, WS_F0 + id1 * 128, epoch);
        const unsigned fo = WS_F0 + (p * 16 + (lane & 15)) * 128;
        poll2(ws, fo, fo, epoch);
      }
      asm volatile("s_barrier" ::: "memory");
      STAMP(2);
      const unsigned vx = tt * 4096 + lane * 16;
      if (w < 2) glds4((unsigned)(WS_R2 + p * 512 + w * 256 + lane * 4), ws, lds0 + P1_R2 + w * 256);
      glds16x4(vx, xg + rot * 16384, lds0 + tt * 4096);
      glds16x4(vx, xg + rot1 * 16384, lds0 + P1_STG + tt * 4096);
      glds16x4(vx, xg + rot2 * 16384, lds0 + 2 * P1_STG + tt * 4096);
      if (cb >= 10) {
        p1_loop<1>(acc, lds, lds0, w, lane, d, xg, rot3);
        STAMP(3);
#pragma unroll
        for (int t = 0; t < 4; ++t) {
          if (t < nq) {
            const int n = (qt0 + t) * 32 + r - 1280;
            const int head = n / 20, dd = n - head * 20;
            const unsigned vp = WS_KV + head * 196608 + blk * 3072 + 1536 + (hh * 24 + dd) * 16;
            ST16(((u32x4){cvtpk(acc[t][0], acc[t][1]), cvtpk(acc[t][2], acc[t][3]), cvtpk(acc[t][4], acc[t][5]), cvtpk(acc[t][6], acc[t][7])}), vp);
            ST16(((u32x4){cvtpk(acc[t][8], acc[t][9]), cvtpk(acc[t][10], acc[t][11]), cvtpk(acc[t][12], acc[t][13]), cvtpk(acc[t][14], acc[t][15])}), vp + 768);
            if (dd >= 18) {
              const unsigned ov = dd == 18 ? 0x3f803f80u : 0u;
              ST16(((u32x4){ov, ov, ov, ov}), vp + 32);
              ST16(((u32x4){ov, ov, ov, ov}), vp + 768 + 32);
            }
          }
        }
      } else {
        p1_loop<0>(acc, lds, lds0, w, lane, d, xg, rot3);
        STAMP(3);
        const bool isq = cb < 5;
        const float rs = isq ? QSCALE : 1.0f;
        const unsigned base = (isq ? WS_Q + blk * 1536 : WS_KV + blk * 3072) + r * 16;
        const unsigned hs = isq ? 64u * 1536u : 196608u;
        const unsigned cst = isq ? 0u : 0x3f80u;
#pragma unroll
        for (int t = 0; t < 4; ++t) {
#pragma unroll
          for (int g = 0; g < 4; g += 2) {
            const auto s0 = __builtin_amdgcn_permlane32_swap(cvtpk(acc[t][4 * g] * rs, acc[t][4 * g + 1] * rs),
                                                             cvtpk(acc[t][4 * g + 4] * rs, acc[t][4 * g + 5] * rs), false, false);
            const auto s1 = __builtin_amdgcn_permlane32_swap(cvtpk(acc[t][4 * g + 2] * rs, acc[t][4 * g + 3] * rs),
                                                             cvtpk(acc[t][4 * g + 6] * rs, acc[t][4 * g + 7] * rs), false, false);
            const unsigned os = (qt0 + t) * 4 + g + hh - (isq ? 0 : 80);
            const unsigned hp = os / 5u, oi = os - hp * 5u;
            const bool sp = oi == 2;
            const unsigned a1 = base + (2 * hp + (oi > 2)) * hs + (oi > 2 ? oi - 3 : oi) * 512;
            ST16(((u32x4){s0[0], s1[0], sp ? cst : s0[1], sp ? 0u : s1[1]}), a1);
            if (sp) ST16(((u32x4){s0[1], s1[1], cst, 0u}), a1 + hs);
          }
        }
      }
      wait_vm<0>();
      unsigned old = 0;
      if (lane == 0) old = __hip_atomic_fetch_add((LDS_AS unsigned*)(LDS_AS char*)&lds[P1_CNT], 1u, __ATOMIC_RELAXED, __HIP_MEMORY_SCOPE_WORKGROUP);
      if (lane == 0 && old == 3) st_flag(ws, WS_F1 + id1 * 128, epoch);
    }
    STAMP(4);
  }
  const int head = xcd * 4 + (i & 3), qb = i >> 2;
  if (w == 0) {
    const int ca = (head * 20) >> 7, cbb = (head * 20 + 19) >> 7;
    const int va = v_colblock(40 + ((head * 20) >> 5)), vb = v_colblock(40 + ((head * 20 + 19) >> 5));
    const int sel = lane >> 4;
    const int c1 = sel == 0 ? 5 + ca : sel == 1 ? 5 + cbb : sel == 2 ? va : vb;
    poll2(ws, WS_F1 + ((lane & 15) * 16 + c1) * 128, WS_F1 + ((2 * qb + (lane & 1)) * 16 + ((lane & 2) ? cbb : ca)) * 128, epoch);
  } else {
#pragma nounroll
    for (int f = b * 7 + (w - 1); f < 2720; f += 1792) {
      const int t = f % 5, kk = (f / 5) % 68, cq = f / 340;
      const int g = lane >> 4, col = cq * 80 + t * 16 + (lane & 15);
      const u16* src; int st4;
      if (kk < 48) { src = w2 + (size_t)(32 * kk + 16 * (g >> 1) + 4 * (g & 1)) * 640; st4 = 8 * 640; }
      else {
        const int ks = 2 * (kk - 48) + (g >> 1);
        if (ks < 32) { src = wo + (size_t)(20 * ks + 4 * (g & 1)) * 640; st4 = 8 * 640; }
        else { src = wo + (size_t)(20 * (4 * (ks - 32) + 2 * (g & 1)) + 16) * 640; st4 = 20 * 640; }
      }
      src += col;
      unsigned e[8];
#pragma unroll
      for (int q = 0; q < 8; ++q) e[q] = src[(q >> 2) * st4 + (q & 3) * 640];
      ST16(((u32x4){e[0] | (e[1] << 16), e[2] | (e[3] << 16), e[4] | (e[5] << 16), e[6] | (e[7] << 16)}), WS_WT + f * 1024 + lane * 16);
    }
  }
  bar_lds();
  STAMP(5);

  {
    const char* kvg = ws + WS_KV + head * 196608;
    const unsigned vdma = (unsigned)(w * 3072 + lane * 16);
    glds16x3(vdma, kvg, lds0 + w * 3072);
    const unsigned qo = WS_Q + (head * 64 + qb * 8 + w) * 1536;
    bf16x8 q0 = LD16(qo + lane * 16, 0);
    u32x4 q1u = __builtin_bit_cast(u32x4, LD16(qo + 1024 + r * 16, 0));
    if (hh) q1u = (u32x4){0u, 0u, 0u, 0u};
    asm volatile("" : "+v"(q0), "+v"(q1u));
    glds16x3(vdma, kvg + P2_STG, lds0 + P2_STG + w * 3072);
    const int dcl = r < 21 ? r : 21;
    const int k1off = hh ? 1536 + 21 * 16 - lane * 16 : 1024;
    const lds_cptr kb = (lds_cptr)lds + lane * 16;
    const lds_cptr vb = (lds_cptr)lds + 1536 + (hh * 24 + dcl) * 16;
    const f32x16 zero = {};
    wait_vm_bar<3>();
    if (tid == 0) st_flag(ws, WS_F1B + b * 128, epoch);
    bf16x8 k0 = lds_b128(kb), k1 = lds_b128(kb + k1off);
    f32x16 sa = MFMA32(k0, q0, zero), sb;
    {
      f32x16 s = MFMA32(k1, __builtin_bit_cast(bf16x8, q1u), sa);
      float m = s[0];
#pragma unroll
      for (int q = 1; q < 16; ++q) m = fmaxf(m, s[q]);
      m = xhalf_max(m);
      q1u[2] = hh ? 0u : (cvtpk(-m, 0.0f) & 0xffffu);
    }
    const bf16x8 q1 = __builtin_bit_cast(bf16x8, q1u);
    sa = MFMA32(k1, q1, sa);
    k0 = lds_b128(kb + 3072); k1 = lds_b128(kb + 3072 + k1off);
    bf16x8 v0 = lds_b128(vb), v1 = {};
    unsigned p[8] = {};
    float e[4] = {};
    f32x16 o = {};
    int sc = 0;
    STAMP(6);
#pragma nounroll
    for (int st = 0; st < 8; ++st) {
      const int sn = sc == 2 * P2_STG ? 0 : sc + P2_STG;
      const lds_cptr kc = kb + sc, kn = kb + sn, vc = vb + sc, vn = vb + sn;
      attn_step<0>(sa, sb, o, q0, q1, k0, k1, v0, v1, p, e, kc, kn, vc, vn, k1off);
      attn_step<1>(sb, sa, o, q0, q1, k0, k1, v0, v1, p, e, kc, kn, vc, vn, k1off);
      attn_step<2>(sa, sb, o, q0, q1, k0, k1, v0, v1, p, e, kc, kn, vc, vn, k1off);
      attn_step<3>(sb, sa, o, q0, q1, k0, k1, v0, v1, p, e, kc, kn, vc, vn, k1off);
      asm volatile("s_waitcnt vmcnt(0)\n\ts_barrier" ::: "memory");
      if (st + 2 < 8) {
        const int sp = sc == 0 ? 2 * P2_STG : sc - P2_STG;
        glds16x3(vdma, kvg + (st + 2) * P2_STG, lds0 + sp + w * 3072);
      }
      SB();
      attn_step<4>(sa, sb, o, q0, q1, k0, k1, v0, v1, p, e, kc, kn, vc, vn, k1off);
      attn_step<5>(sb, sa, o, q0, q1, k0, k1, v0, v1, p, e, kc, kn, vc, vn, k1off);
      attn_step<6>(sa, sb, o, q0, q1, k0, k1, v0, v1, p, e, kc, kn, vc, vn, k1off);
      attn_step<7>(sb, sa, o, q0, q1, k0, k1, v0, v1, p, e, kc, kn, vc, vn, k1off);
      sc = sn;
    }
    STAMP(7);
    p[6] = cvtpk(e[0], e[1]); p[7] = cvtpk(e[2], e[3]);
    o = MFMA32(v1, PK4(p[4], p[5], p[6], p[7]), o);
    float l = upper_half(o[8]);
    float amax = 0.f;
#pragma unroll
    for (int q = 0; q < 12; ++q) amax = fmaxf(amax, fabsf(o[q]));
    const bool bad = !(l < 1.0e9f) || !(l > 0.25f) || !(amax < 1.0e30f);
    if (__builtin_expect(__any(bad), 0)) {
      q1u[2] = 0u;
      const bf16x8 q1z = __builtin_bit_cast(bf16x8, q1u);
      float m = -INFINITY;
      o = zero;
#pragma nounroll
      for (int t = 0; t < 64; ++t) {
        const unsigned tp = WS_KV + head * 196608 + t * 3072;
        f32x16 s = MFMA32(LD16(tp + lane * 16, 0), q0, zero);
        s = MFMA32(LD16(tp + k1off + lane * 16, 0), q1z, s);
        float tm = s[0];
#pragma unroll
        for (int q = 1; q < 16; ++q) tm = fmaxf(tm, s[q]);
        tm = xhalf_max(tm);
        const float mn = fmaxf(m, tm);
        const float al = EXP2(m - mn);
        m = mn;
        unsigned pk[8];
#pragma unroll
        for (int q = 0; q < 8; ++q) pk[q] = cvtpk(EXP2(s[2 * q] - mn), EXP2(s[2 * q + 1] - mn));
#pragma unroll
        for (int q = 0; q < 16; ++q) o[q] *= al;
        const unsigned vp = tp + 1536 + (hh * 24 + dcl) * 16;
        o = MFMA32(LD16(vp, 0), PK4(pk[0], pk[1], pk[2], pk[3]), o);
        o = MFMA32(LD16(vp + 768, 0), PK4(pk[4], pk[5], pk[6], pk[7]), o);
      }
      l = upper_half(o[8]);
    }
    const float il = 1.0f / l;
    const unsigned op = WS_O + (qb * 8 + w) * 40 * 1024;
    ST16(((u32x4){cvtpk(o[0] * il, o[1] * il), cvtpk(o[2] * il, o[3] * il), cvtpk(o[4] * il, o[5] * il), cvtpk(o[6] * il, o[7] * il)}),
         op + head * 1024 + lane * 16);
    if (hh == 0)
      ST8(((u32x2){cvtpk(o[8] * il, o[9] * il), cvtpk(o[10] * il, o[11] * il)}), op + (32 + (head >> 2)) * 1024 + (((head >> 1) & 1) * 32 + r) * 16 + (head & 1) * 8);
    if (w < 2) poll2(ws, WS_F1B + (w * 128 + lane) * 128, WS_F1B + (w * 128 + 64 + lane) * 128, epoch);
    wait_vm_bar<0>();
    if (tid == 0) {
      st_flag(ws, WS_F2 + (qb * 32 + head) * 128, epoch);
      if (b == 0) st_flag(ws, WS_SYNC, epoch);
    }
    STAMP(8);
  }

  {
    const int rb = xcd * 4 + (i & 3), cq = i >> 2;
    const int g = lane >> 4, c = lane & 15;
    const unsigned ao = (g >> 1) * 1024 + ((g & 1) * 32 + c) * 16;
    const unsigned wofs = WS_WT + cq * 68 * 5 * 1024 + lane * 16;
    f32x4 acc[4][5] = {};
    bf16x8 af[3][4], wf[3][5];
    const int no = w < 4 ? 3 : 2, ko0 = w < 4 ? 3 * w : 12 + 2 * (w - 4);
    auto ldh = [&](int s, int kk) {
#pragma unroll
      for (int m = 0; m < 4; ++m) af[s][m] = LD16(ao + (m & 1) * 256, WS_H + ((2 * rb + (m >> 1)) * 96 + 2 * kk) * 1024);
#pragma unroll
      for (int t = 0; t < 5; ++t) wf[s][t] = LD16(wofs + t * 1024, kk * 5120);
    };
    auto ldo = [&](int s, int ko) {
#pragma unroll
      for (int m = 0; m < 4; ++m) af[s][m] = LD16(ao + (m & 1) * 256, WS_O + ((2 * rb + (m >> 1)) * 40 + 2 * ko) * 1024);
#pragma unroll
      for (int t = 0; t < 5; ++t) wf[s][t] = LD16(wofs + t * 1024, (48 + ko) * 5120);
    };
    auto mm = [&](int s) {
      SB();
#pragma unroll
      for (int m = 0; m < 4; ++m)
#pragma unroll
        for (int t = 0; t < 5; ++t) acc[m][t] = MFMA16(wf[s][t], af[s][m], acc[m][t]);
      SB();
    };
    ldh(0, 6 * w); ldh(1, 6 * w + 1); ldh(2, 6 * w + 2);
    mm(0); ldh(0, 6 * w + 3);
    mm(1); ldh(1, 6 * w + 4);
    mm(2); ldh(2, 6 * w + 5);
    mm(0);
    {
      const unsigned fo = WS_F2 + ((rb >> 2) * 32 + (lane & 31)) * 128;
      poll2(ws, fo, fo, epoch);
    }
    STAMP(9);
    ldo(0, ko0);
    mm(1); ldo(1, ko0 + 1);
    mm(2); ldo(2, min(ko0 + 2, 19));
    mm(0); mm(1);
    if (no == 3) mm(2);
    STAMP(10);
    LDS_AS f32x4* red = (LDS_AS f32x4*)(LDS_AS char*)lds;
#pragma unroll
    for (int m = 0; m < 4; ++m)
#pragma unroll
      for (int t = 0; t < 5; ++t) red[(w * 20 + m * 5 + t) * 64 + lane] = acc[m][t];
    bar_lds();
    STAMP(11);
#pragma nounroll
    for (int tl = w; tl < 20; tl += 8) {
      const int m = tl / 5, t = tl - m * 5;
      const size_t go = (size_t)(rb * 64 + m * 16 + c) * 640 + cq * 80 + t * 16 + 4 * g;
      const u32x2 xv = *(const u32x2*)(x + go);
      const u32x2 bv = *(const u32x2*)(b2 + cq * 80 + t * 16 + 4 * g);
      f32x4 a = red[tl * 64 + lane];
#pragma unroll
      for (int q = 1; q < 8; ++q) a += red[(q * 20 + tl) * 64 + lane];
      *(u32x2*)(out + go) = (u32x2){cvtpk(a[0] + bflo(xv[0]) + bflo(bv[0]), a[1] + bfhi(xv[0]) + bfhi(bv[0])),
                                    cvtpk(a[2] + bflo(xv[1]) + bflo(bv[1]), a[3] + bfhi(xv[1]) + bfhi(bv[1]))};
    }
    STAMP(12);
  }
}

extern "C" void kernel_launch(void* const* d_in, const int* in_sizes, int n_in, void* d_out, int out_size, void* d_ws, size_t ws_size,
                              hipStream_t stream) {
  mega_kernel<<<256, 512, 0, stream>>>((const u16*)d_in[0], (const u16*)d_in[1], (const u16*)d_in[2], (const u16*)d_in[4], (const u16*)d_in[5],
                                       (const u16*)d_in[6], (const u16*)d_in[7], (char*)d_ws, (u16*)d_out);
}
```

```cpp
#include <hip/hip_runtime.h>
#include <hip/hip_bf16.h>
#include <stdint.h>

typedef unsigned short u16;
typedef unsigned long long u64;
typedef short bf16x8 __attribute__((ext_vector_type(8)));
typedef short s16x4 __attribute__((ext_vector_type(4)));
typedef float f32x16 __attribute__((ext_vector_type(16)));
typedef float f32x4 __attribute__((ext_vector_type(4)));
typedef float f32x2 __attribute__((ext_vector_type(2)));
typedef unsigned u32x4 __attribute__((ext_vector_type(4)));
typedef unsigned u32x2 __attribute__((ext_vector_type(2)));
typedef __bf16 bf2 __attribute__((ext_vector_type(2)));
#define LDS_AS __attribute__((address_space(3)))
#define GAS __attribute__((address_space(1)))
typedef LDS_AS const char* lds_cptr;
typedef GAS u64 gu64;
#define RLX_AGENT __ATOMIC_RELAXED, __HIP_MEMORY_SCOPE_AGENT

constexpr float EPS = 1e-5f;
constexpr float QSCALE = 0.22360679774997896f * 1.4426950408889634f;

constexpr unsigned WS_SYNC = 0;
constexpr unsigned WS_F0 = 4096, WS_F1 = WS_F0 + 32768, WS_F1B = WS_F1 + 32768, WS_F2 = WS_F1B + 32768;
constexpr unsigned WS_Q = WS_F2 + 32768;
constexpr unsigned WS_KV = WS_Q + 32 * 64 * 1536;
constexpr unsigned WS_O = WS_KV + 32 * 196608;
constexpr unsigned WS_H = WS_O + 64 * 40 * 1024;
constexpr unsigned WS_XN = WS_H + 64 * 96 * 1024;
constexpr unsigned WS_RS = WS_XN;
constexpr unsigned WS_WT = WS_RS + 32768;
constexpr unsigned WS_END = WS_WT + 8 * 68 * 5 * 1024;

#define MFMA32(a, b, c) __builtin_amdgcn_mfma_f32_32x32x16_bf16(a, b, c, 0, 0, 0)
#define MFMA16(a, b, c) __builtin_amdgcn_mfma_f32_16x16x32_bf16(a, b, c, 0, 0, 0)
#define SB() __builtin_amdgcn_sched_barrier(0)
#define EXP2(v) __builtin_amdgcn_exp2f(v)
#define PK4(a, b, c, d) __builtin_bit_cast(bf16x8, ((u32x4){a, b, c, d}))
#define ST16(v, off) __builtin_amdgcn_raw_buffer_store_b128(v, wsr, off, 0, 16)
#define ST8(v, off) __builtin_amdgcn_raw_buffer_store_b64(v, wsr, off, 0, 16)
#define LD16(off, soff) __builtin_bit_cast(bf16x8, __builtin_amdgcn_raw_buffer_load_b128(wsr, off, soff, 16))

#define STAMP(i)

__device__ __forceinline__ unsigned cvtpk(float lo, float hi) {
  return __builtin_bit_cast(unsigned, __builtin_convertvector((f32x2){lo, hi}, bf2));
}
__device__ __forceinline__ float bflo(unsigned u) { return __uint_as_float(u << 16); }
__device__ __forceinline__ float bfhi(unsigned u) { return __uint_as_float(u & 0xffff0000u); }
__device__ __forceinline__ void glds4(unsigned voff, const void* sbase, unsigned ldsb) {
  unsigned sv;
  asm volatile("s_mov_b32 %0, m0\n\ts_mov_b32 m0, %3\n\ts_nop 0\n\tglobal_load_lds_dword %1, %2 sc1\n\ts_mov_b32 m0, %0"
               : "=&s"(sv) : "v"(voff), "s"(sbase), "s"(ldsb) : "memory");
}
__device__ __forceinline__ void glds16x3(unsigned voff, const void* sbase, unsigned ldsb) {
  unsigned sv;
  asm volatile("s_mov_b32 %0, m0\n\ts_mov_b32 m0, %3\n\ts_nop 0\n\t"
               "global_load_lds_dwordx4 %1, %2 sc1\n\t"
               "global_load_lds_dwordx4 %1, %2 offset:1024 sc1\n\t"
               "global_load_lds_dwordx4 %1, %2 offset:2048 sc1\n\t"
               "s_mov_b32 m0, %0"
               : "=&s"(sv) : "v"(voff), "s"(sbase), "s"(ldsb) : "memory");
}
__device__ __forceinline__ void glds16x4(unsigned voff, const void* sbase, unsigned ldsb) {
  unsigned sv;
  asm volatile("s_mov_b32 %0, m0\n\ts_mov_b32 m0, %3\n\ts_nop 0\n\t"
               "global_load_lds_dwordx4 %1, %2 sc1\n\t"
               "global_load_lds_dwordx4 %1, %2 offset:1024 sc1\n\t"
               "global_load_lds_dwordx4 %1, %2 offset:2048 sc1\n\t"
               "global_load_lds_dwordx4 %1, %2 offset:3072 sc1\n\t"
               "s_mov_b32 m0, %0"
               : "=&s"(sv) : "v"(voff), "s"(sbase), "s"(ldsb) : "memory");
}
template <int N> __device__ __forceinline__ void wait_vm() { asm volatile("s_waitcnt vmcnt(%0)" ::"i"(N) : "memory"); }
template <int N> __device__ __forceinline__ void wait_vm_bar() {
  __builtin_amdgcn_s_waitcnt(0xC07F);
  asm volatile("s_waitcnt vmcnt(%0) lgkmcnt(0)\n\ts_barrier" ::"i"(N) : "memory");
}
__device__ __forceinline__ void bar_lds() { asm volatile("s_waitcnt lgkmcnt(0)\n\ts_barrier" ::: "memory"); }
__device__ __forceinline__ bf16x8 lds_b128(lds_cptr p) { return *(const LDS_AS bf16x8*)p; }
__device__ __forceinline__ s16x4 lds_tr(lds_cptr p) { return __builtin_amdgcn_ds_read_tr16_b64_v4i16((LDS_AS s16x4*)p); }
__device__ __forceinline__ bf16x8 cat8(s16x4 a, s16x4 b) { return __builtin_shufflevector(a, b, 0, 1, 2, 3, 4, 5, 6, 7); }
__device__ __forceinline__ float xhalf_max(float v) {
  auto rr = __builtin_amdgcn_permlane32_swap(__float_as_uint(v), __float_as_uint(v), false, false);
  return fmaxf(__uint_as_float(rr[0]), __uint_as_float(rr[1]));
}
__device__ __forceinline__ float upper_half(float v) {
  auto rr = __builtin_amdgcn_permlane32_swap(__float_as_uint(v), __float_as_uint(v), false, false);
  return __uint_as_float(rr[1]);
}
__device__ __forceinline__ float gelu_tanh(float x) {
  const float c0 = -2.0f * 0.7978845608028654f * 1.4426950408889634f;
  const float c1 = c0 * 0.044715f;
  float t = fmaf(x * x, c1, c0);
  float e = __builtin_amdgcn_exp2f(x * t);
  return x * __builtin_amdgcn_rcpf(1.0f + e);
}
__device__ __forceinline__ unsigned gelu2(f32x2 x) {
  const float c0 = -2.0f * 0.7978845608028654f * 1.4426950408889634f;
  const f32x2 k0 = {c0, c0}, k1 = {c0 * 0.044715f, c0 * 0.044715f}, one = {1.0f, 1.0f};
  const f32x2 z = x * __builtin_elementwise_fma(x * x, k1, k0);
  const f32x2 d = (f32x2){__builtin_amdgcn_exp2f(z[0]), __builtin_amdgcn_exp2f(z[1])} + one;
  const f32x2 g = x * (f32x2){__builtin_amdgcn_rcpf(d[0]), __builtin_amdgcn_rcpf(d[1])};
  return cvtpk(g[0], g[1]);
}
template <int CTRL, int RMASK> __device__ __forceinline__ float dpp_add(float v) {
  return v + __uint_as_float(__builtin_amdgcn_update_dpp(0u, __float_as_uint(v), CTRL, RMASK, 0xf, false));
}
__device__ __forceinline__ float wave_sum(float v) {
  v = dpp_add<0x111, 0xf>(v); v = dpp_add<0x112, 0xf>(v); v = dpp_add<0x114, 0xf>(v); v = dpp_add<0x118, 0xf>(v);
  v = dpp_add<0x142, 0xa>(v); v = dpp_add<0x143, 0xc>(v);
  return __uint_as_float(__builtin_amdgcn_readlane(__float_as_uint(v), 63));
}
__device__ __forceinline__ u64 ld_flag(const char* ws, unsigned off) { return __hip_atomic_load((gu64*)(ws + off), RLX_AGENT); }
__device__ __forceinline__ void st_flag(char* ws, unsigned off, u64 v) { __hip_atomic_store((gu64*)(ws + off), v, RLX_AGENT); }
__device__ __forceinline__ u64 rfl64(u64 v) {
  return ((u64)(unsigned)__builtin_amdgcn_readfirstlane((unsigned)(v >> 32)) << 32) | (unsigned)__builtin_amdgcn_readfirstlane((unsigned)v);
}
constexpr unsigned SPIN_CAP = 1u << 18;
__device__ __forceinline__ void poll2(const char* ws, unsigned off1, unsigned off2, u64 epoch) {
  u64 a, b, m; unsigned c;
  asm volatile("s_mov_b32 %3, %8\n"
               ".Lpl%=:\n\t"
               "global_load_dwordx2 %0, %4, %6 sc1\n\t"
               "global_load_dwordx2 %1, %5, %6 sc1\n\t"
               "s_waitcnt vmcnt(0)\n\t"
               "v_cmp_ne_u64 %2, %7, %0\n\t"
               "v_cmp_ne_u64 vcc, %7, %1\n\t"
               "s_nop 1\n\t"
               "s_or_b64 %2, %2, vcc\n\t"
               "s_cbranch_scc0 .Lpd%=\n\t"
               "s_sleep 3\n\t"
               "s_sub_u32 %3, %3, 1\n\t"
               "s_cbranch_scc0 .Lpl%=\n"
               ".Lpd%=:"
               : "=&v"(a), "=&v"(b), "=&s"(m), "=&s"(c) : "v"(off1), "v"(off2), "s"(ws), "s"(epoch), "i"(SPIN_CAP) : "memory", "vcc", "scc");
}
__device__ __forceinline__ void lds_wait(unsigned la, unsigned want) {
  unsigned v, sv, c;
  asm volatile("s_mov_b32 %2, %5\n"
               ".Lwl%=:\n\t"
               "ds_read_b32 %0, %3\n\t"
               "s_waitcnt lgkmcnt(0)\n\t"
               "v_readfirstlane_b32 %1, %0\n\t"
               "s_nop 1\n\t"
               "s_cmp_ge_u32 %1, %4\n\t"
               "s_cbranch_scc1 .Lwd%=\n\t"
               "s_sleep 2\n\t"
               "s_sub_u32 %2, %2, 1\n\t"
               "s_cbranch_scc0 .Lwl%=\n"
               ".Lwd%=:"
               : "=&v"(v), "=&s"(sv), "=&s"(c) : "v"(la), "s"(want), "i"(SPIN_CAP * 8) : "memory", "scc");
}

constexpr int P1_SLOT = 32768, P1_W = 16384;
constexpr int P1_TOP = 4 * P1_SLOT;
constexpr int P1_B1 = P1_TOP + 24576, P1_CNT = P1_B1 + 256, P1_TCH = P1_CNT + 256, P1_CSA = P1_TCH + 256, P1_RSV = P1_CSA + 2048;
constexpr int LDS_BYTES = 163840;
static_assert(P1_RSV + 2048 <= LDS_BYTES, "lds");
constexpr unsigned OOB = 0x7ffffff0u;

__device__ __forceinline__ void glds16(unsigned voff, const void* sbase, unsigned ldsb) {
  unsigned sv;
  asm volatile("s_mov_b32 %0, m0\n\ts_mov_b32 m0, %3\n\ts_nop 0\n\tglobal_load_lds_dwordx4 %1, %2 sc1\n\ts_mov_b32 m0, %0"
               : "=&s"(sv) : "v"(voff), "s"(sbase), "s"(ldsb) : "memory");
}
__device__ __forceinline__ float addf(float a, float b) { float r; asm("v_add_f32_e32 %0, %1, %2" : "=v"(r) : "v"(a), "v"(b)); return r; }
__device__ __forceinline__ void colsum(float& lo, float& hi, const bf16x8& f) {
  const u32x4 u = __builtin_bit_cast(u32x4, f);
#pragma unroll
  for (int e = 0; e < 4; ++e) { lo = addf(lo, bflo(u[e])); hi = addf(hi, bfhi(u[e])); }
  asm volatile("" : "+v"(lo), "+v"(hi));
}
template <int E> __device__ __forceinline__ void colsum1(float& lo, float& hi, const bf16x8& f) {
  const unsigned u = __builtin_bit_cast(u32x4, f)[E];
  lo = addf(lo, bflo(u)); hi = addf(hi, bfhi(u));
  asm volatile("" : "+v"(lo), "+v"(hi));
}
__device__ __forceinline__ bf16x8 split_a(float v) {
  const unsigned h = cvtpk(v, 0.f) & 0xffffu, pk = h | (cvtpk(v - bflo(h), 0.f) << 16);
  return PK4(pk, pk, 0u, 0u);
}
__device__ __forceinline__ bf16x8 split_b(float v) {
  const unsigned h = cvtpk(v, v);
  const float l = v - bflo(h);
  return PK4(h, cvtpk(l, l), 0u, 0u);
}
struct P1Dma {
  unsigned vo[4], vm[3];
  const char *base, *wm;
  unsigned kstride;
  unsigned ldsw, lstep, ldsm;
};
template <int S, int J> __device__ __forceinline__ void p1_piece(const P1Dma& d) {
  if (S < 10) glds16(d.vo[J], d.base + S * d.kstride, d.ldsw + (S & 3) * P1_SLOT + J * d.lstep);
  else if (J < 3) glds16(d.vm[J < 3 ? J : 0], d.wm + 2 * (S - 10) * (64 * 3072), d.ldsm + (S == 14 ? P1_TOP : (S & 3) * P1_SLOT) + J * 4096);
}
template <int S> __device__ __forceinline__ void p1_issue(const P1Dma& d) { p1_piece<S, 0>(d); p1_piece<S, 1>(d); p1_piece<S, 2>(d); p1_piece<S, 3>(d); }

struct FragA { bf16x8 x0[2], x1[4], w[4]; };
struct PtrA { lds_cptr x0[4], x1[4], w[4]; LDS_AS float* csw; const char *ws, *tb; unsigned g0, g1, tv, tl; };
struct StatA { float lo, hi; u32x4 gk, go; };
template <int S, int J> __device__ __forceinline__ void rd_a(FragA& f, bf16x8 (&xk)[20], const PtrA& p) {
  constexpr int so = (S & 3) * P1_SLOT;
  if (J < 2) xk[2 * S + (J & 1)] = lds_b128(p.x0[J] + so); else f.x0[J & 1] = lds_b128(p.x0[J] + so);
  f.w[J] = cat8(lds_tr(p.w[J] + so), lds_tr(p.w[J] + so + 1024));
  f.x1[J] = lds_b128(p.x1[J] + so);
}
template <bool VT> __device__ __forceinline__ void mm_a(f32x16& acc, const bf16x8& xf, const bf16x8& wf) {
  SB(); acc = VT ? MFMA32(xf, wf, acc) : MFMA32(wf, xf, acc); SB();
}
template <bool VT, int S>
__device__ __forceinline__ void p1_stage_a(f32x16 (&acc)[2], FragA& fn, const FragA& fc, bf16x8 (&xk)[20], const PtrA& p, const P1Dma& d, StatA& st) {
  wait_vm_bar<(S == 0 ? 8 : S < 3 ? 9 : S == 3 ? 12 : S < 6 ? 11 : S == 6 ? 8 : S == 7 ? 9 : S == 8 ? 8 : 6)>();
  if (S == 9) asm volatile("" : "+v"(st.gk), "+v"(st.go));
#define CS(E, J) if (S < 9) colsum1<E>(st.lo, st.hi, fc.w[J])
  mm_a<VT>(acc[0], xk[2 * S], fc.w[0]);     if (S < 9) rd_a<(S + 1) % 10, 0>(fn, xk, p); CS(0, 0);
  mm_a<VT>(acc[1], fc.x1[0], fc.w[0]);      if (S < 9) rd_a<(S + 1) % 10, 2>(fn, xk, p); CS(1, 0);
  mm_a<VT>(acc[0], xk[2 * S + 1], fc.w[1]); p1_piece<S + 4, 0>(d); CS(2, 0);
  mm_a<VT>(acc[1], fc.x1[1], fc.w[1]);      if (S < 9) rd_a<(S + 1) % 10, 1>(fn, xk, p); CS(3, 0);
  mm_a<VT>(acc[0], fc.x0[0], fc.w[2]);      p1_piece<S + 4, 1>(d); CS(0, 2);
  mm_a<VT>(acc[1], fc.x1[2], fc.w[2]);      if (S < 9) rd_a<(S + 1) % 10, 3>(fn, xk, p); CS(1, 2);
  mm_a<VT>(acc[0], fc.x0[1], fc.w[3]);      p1_piece<S + 4, 2>(d); CS(2, 2);
  mm_a<VT>(acc[1], fc.x1[3], fc.w[3]);      p1_piece<S + 4, 3>(d); CS(3, 2);
#undef CS
  if (S == 0) glds4(p.tv, p.tb, p.tl);
  if (S == 2) p1_issue<14>(d);
  if (S == 6)
    asm volatile("global_load_dwordx4 %0, %2, %4 sc1\n\tglobal_load_dwordx4 %1, %3, %4 sc1" : "=&v"(st.gk), "=&v"(st.go) : "v"(p.g0), "v"(p.g1), "s"(p.ws) : "memory");
  if (S == 8) { colsum(st.lo, st.hi, fn.w[0]); colsum(st.lo, st.hi, fn.w[2]); st.lo += st.hi; *p.csw = st.lo; }
}
template <bool VT>
__device__ __forceinline__ void p1_pass_a(f32x16 (&acc)[2], bf16x8 (&xk)[20], const PtrA& p, const P1Dma& d, StatA& st) {
  FragA f0, f1;
  wait_vm_bar<13>();
  rd_a<0, 0>(f0, xk, p); rd_a<0, 2>(f0, xk, p); rd_a<0, 1>(f0, xk, p); rd_a<0, 3>(f0, xk, p);
  p1_stage_a<VT, 0>(acc, f1, f0, xk, p, d, st); p1_stage_a<VT, 1>(acc, f0, f1, xk, p, d, st);
  p1_stage_a<VT, 2>(acc, f1, f0, xk, p, d, st); p1_stage_a<VT, 3>(acc, f0, f1, xk, p, d, st);
  p1_stage_a<VT, 4>(acc, f1, f0, xk, p, d, st); p1_stage_a<VT, 5>(acc, f0, f1, xk, p, d, st);
  p1_stage_a<VT, 6>(acc, f1, f0, xk, p, d, st); p1_stage_a<VT, 7>(acc, f0, f1, xk, p, d, st);
  p1_stage_a<VT, 8>(acc, f1, f0, xk, p, d, st); p1_stage_a<VT, 9>(acc, f0, f1, xk, p, d, st);
}
struct FragB { bf16x8 w[2][3]; };
template <int K, int J, int T> __device__ __forceinline__ void rd_b(FragB& f, const lds_cptr (&wpb)[2][3]) {
  constexpr int slot = K >= 8 ? P1_TOP : ((10 + K / 2) & 3) * P1_SLOT;
  constexpr int off = slot + (K & 1) * 12288 + T * 64;
  f.w[J][T] = cat8(lds_tr(wpb[J][off >> 16] + (off & 0xffff)), lds_tr(wpb[J][off >> 16] + (off & 0xffff) + 768));
}
template <int K>
__device__ __forceinline__ void p1_stage_b(f32x16 (&acc)[3], FragB& fn, const FragB& fc, const bf16x8 (&xk)[20], const lds_cptr (&wpb)[2][3],
                                           float (&cb)[6], int par) {
  constexpr int N = K < 9 ? K + 1 : 0;
  mm_a<false>(acc[0], xk[2 * K], fc.w[0][0]);     if (K < 9) { rd_b<N, 0, 0>(fn, wpb); rd_b<N, 0, 1>(fn, wpb); }
  mm_a<false>(acc[1], xk[2 * K], fc.w[0][1]);     if (K < 9) rd_b<N, 0, 2>(fn, wpb);
  mm_a<false>(acc[2], xk[2 * K], fc.w[0][2]);     if (K < 9) rd_b<N, 1, 0>(fn, wpb);
  mm_a<false>(acc[0], xk[2 * K + 1], fc.w[1][0]); if (K < 9) rd_b<N, 1, 1>(fn, wpb);
  mm_a<false>(acc[1], xk[2 * K + 1], fc.w[1][1]); if (K < 9) rd_b<N, 1, 2>(fn, wpb);
  mm_a<false>(acc[2], xk[2 * K + 1], fc.w[1][2]);
  if (par == (K & 1)) { colsum(cb[0], cb[1], fc.w[0][0]); colsum(cb[2], cb[3], fc.w[0][1]); colsum(cb[4], cb[5], fc.w[0][2]); }
}
template <int N> __device__ __forceinline__ void vm_bar() { asm volatile("s_waitcnt vmcnt(%0)\n\ts_barrier" ::"i"(N) : "memory"); }

constexpr int P2_STG = 24576;
constexpr int P3_WO = 81920;
constexpr int P3_C2 = P1_CNT + 16;
template <int T, class HK>
__device__ __forceinline__ void attn_step(HK&& hook, f32x16& Sc, f32x16& Sn, f32x16& o, const bf16x8& q0, const bf16x8& q1, bf16x8& k0, bf16x8& k1,
                                          bf16x8& v0, bf16x8& v1, unsigned (&p)[8], float (&e)[4], lds_cptr kc, lds_cptr kn, lds_cptr vc,
                                          lds_cptr vn, int k1off) {
  const f32x16 zero = {};
  const lds_cptr k2p = (T + 2 < 8) ? kc + (T + 2) * 3072 : kn + (T + 2 - 8) * 3072;
  const lds_cptr v1p = (T + 1 < 8) ? vc + (T + 1) * 3072 : vn;
  float a0, a1, a2, a3;
  Sn = MFMA32(k0, q0, zero);
  k0 = lds_b128(k2p);
  hook(2 * T);
  SB();
  p[6] = cvtpk(e[0], e[1]); p[7] = cvtpk(e[2], e[3]);
  SB();
  a0 = EXP2(Sc[0]); a1 = EXP2(Sc[1]); a2 = EXP2(Sc[2]); a3 = EXP2(Sc[3]);
  SB();
  o = MFMA32(v1, PK4(p[4], p[5], p[6], p[7]), o);
  v1 = lds_b128(vc + T * 3072 + 768);
  SB();
  p[0] = cvtpk(a0, a1); p[1] = cvtpk(a2, a3);
  SB();
  a0 = EXP2(Sc[4]); a1 = EXP2(Sc[5]); a2 = EXP2(Sc[6]); a3 = EXP2(Sc[7]);
  SB();
  Sn = MFMA32(k1, q1, Sn);
  k1 = lds_b128(k2p + k1off);
  hook(2 * T + 1);
  SB();
  p[2] = cvtpk(a0, a1); p[3] = cvtpk(a2, a3);
  SB();
  a0 = EXP2(Sc[8]); a1 = EXP2(Sc[9]); a2 = EXP2(Sc[10]); a3 = EXP2(Sc[11]);
  SB();
  o = MFMA32(v0, PK4(p[0], p[1], p[2], p[3]), o);
  v0 = lds_b128(v1p);
  SB();
  p[4] = cvtpk(a0, a1); p[5] = cvtpk(a2, a3);
  SB();
  e[0] = EXP2(Sc[12]); e[1] = EXP2(Sc[13]); e[2] = EXP2(Sc[14]); e[3] = EXP2(Sc[15]);
  SB();
}

__device__ __forceinline__ int v_colblock(int tile) { return tile < 48 ? tile >> 2 : 12 + (tile - 48) / 3; }

__global__ __launch_bounds__(512, 2) void mega_kernel(const u16* __restrict__ x, const u16* __restrict__ wqkv, const u16* __restrict__ wo,
                                                       const u16* __restrict__ w1, const u16* __restrict__ b1, const u16* __restrict__ w2,
                                                       const u16* __restrict__ b2, char* ws, u16* __restrict__ out) {
  __shared__ __attribute__((aligned(16))) char lds[LDS_BYTES];
  const unsigned lds0 = (unsigned)(uintptr_t)(LDS_AS char*)lds;
  const int b = blockIdx.x, xcd = b & 7, i = b >> 3;
  const int tid = threadIdx.x, lane = tid & 63, w = __builtin_amdgcn_readfirstlane(tid >> 6);
  const int r = lane & 31, hh = lane >> 5, tt = w & 3;
  const auto wsr = __builtin_amdgcn_make_buffer_rsrc(ws, 0, WS_END, 0x00020000);
  STAMP(0);
  const int head = xcd * 4 + (i & 3), qb = i >> 2;
  const int p = (xcd & 1) * 8 + (i & 7), cb = (xcd >> 1) * 4 + (i >> 3);
  const int id1 = p * 16 + cb;
  u64 epoch_v; u32x4 xa, xb;
  {
    const unsigned xo = (unsigned)((p * 128 + cb * 8 + w) * 1280);
    asm volatile("global_load_dwordx2 %0, %3, %6 sc1\n\tglobal_load_dwordx4 %1, %4, %7\n\tglobal_load_dwordx4 %2, %5, %7"
                 : "=&v"(epoch_v), "=&v"(xa), "=&v"(xb) : "v"(WS_SYNC), "v"(xo + lane * 16), "v"(xo + 1024 + (lane & 15) * 16), "s"(ws), "s"(x) : "memory");
  }
  if (tid < 8) ((LDS_AS unsigned*)(LDS_AS char*)&lds[P1_CNT])[tid] = 0u;
  u64 epoch;
  const char* kvg = ws + WS_KV + head * 196608;
  const unsigned vdma = (unsigned)(w * 3072 + lane * 16);
  const unsigned qo = WS_Q + (head * 64 + qb * 8 + w) * 1536;
  bf16x8 q0; u32x4 q1u;

  {
    const int qt0 = cb < 12 ? cb * 4 : 48 + (cb - 12) * 3;
    const int nq = cb < 12 ? 4 : 3;
    const int wh = w >> 2, nt = w >> 1, tp = w & 1;
    const int tk = 2 * tp + (nt & 1), to = tk ^ 1;
    P1Dma d;
    if (w >= 4) {
      const int qr = 4 * tt + (lane >> 4);
#pragma unroll
      for (int q = 0; q < 4; ++q)
        d.vo[q] = (unsigned)((qr + 16 * q) * 3840 + (qt0 + min((((lane >> 2) & 3) ^ (qr & 3)), nq - 1)) * 64 + (lane & 3) * 16);
      d.base = (const char*)wqkv; d.kstride = 64 * 3840; d.ldsw = lds0 + P1_W + tt * 1024; d.lstep = 4096;
    } else {
#pragma unroll
      for (int q = 0; q < 4; ++q) {
        const int row = 32 * tt + 8 * q + (lane >> 3);
        d.vo[q] = (unsigned)((p * 128 + row) * 1280 + (((lane & 7) ^ ((row >> 1) & 7)) << 4));
      }
      d.base = (const char*)x; d.kstride = 128; d.ldsw = lds0 + tt * 4096; d.lstep = 1024;
    }
    if (w == 7) glds4((unsigned)(cb * 192 + min(lane, 47) * 4), b1, lds0 + P1_B1);
    p1_issue<0>(d);
    d.wm = (const char*)w1 + wh * (64 * 3072); d.ldsm = lds0 + wh * 12288 + tt * 1024;
#pragma unroll
    for (int q = 0; q < 3; ++q) { const int u = q * 256 + tt * 64 + lane; d.vm[q] = (unsigned)((u / 12) * 3072 + cb * 192 + (u % 12) * 16); }
    asm volatile("s_waitcnt vmcnt(4)" : "+v"(epoch_v), "+v"(xa), "+v"(xb)::"memory");
    epoch = rfl64(epoch_v) + 1;
    const unsigned tag = (unsigned)epoch ^ (unsigned)(epoch >> 32);
    {
      const float c = __uint_as_float(__builtin_amdgcn_readfirstlane(xa[0]) << 16);
      float s1 = 0.f, s2 = 0.f, t1 = 0.f, t2 = 0.f;
#pragma unroll
      for (int e = 0; e < 4; ++e) {
        const float a0 = bflo(xa[e]) - c, a1 = bfhi(xa[e]) - c, b0 = bflo(xb[e]) - c, b1v = bfhi(xb[e]) - c;
        s1 += a0 + a1; s2 = fmaf(a0, a0, fmaf(a1, a1, s2)); t1 += b0 + b1v; t2 = fmaf(b0, b0, fmaf(b1v, b1v, t2));
      }
      if (lane < 16) { s1 += t1; s2 += t2; }
      const float m = wave_sum(s1) * (1.0f / 640.0f);
      const float var = fmaxf(wave_sum(s2) * (1.0f / 640.0f) - m * m, 0.0f);
      const float r1 = __builtin_amdgcn_rsqf(var + EPS);
      const float r2 = __builtin_amdgcn_rsqf(var * r1 * r1 + EPS);
      ST16(((u32x4){__float_as_uint(m + c), __float_as_uint(r1), __float_as_uint(r1 * r2), tag}), lane == 0 ? WS_RS + (p * 128 + cb * 8 + w) * 16 : OOB);
    }
    STAMP(1);
    p1_issue<1>(d); p1_issue<2>(d); p1_issue<3>(d);
    PtrA pa;
    {
      if (w < 3) {
        const int v = w * 64 + lane;
        pa.tv = (unsigned)((p * 128 + (i >> 3) * 32 + min(v / 5, 31)) * 1280 + 640 + (v % 5) * 128); pa.tb = (const char*)x;
      } else if (w < 5) {
        const int v = (w - 3) * 64 + lane;
        pa.tv = (unsigned)((320 + (i & 7) * 40 + min(v / 3, 39)) * 3840 + min(qt0 * 64 + (v % 3) * 124, 3836)); pa.tb = (const char*)wqkv;
      } else {
        const int v = (w - 5) * 64 + lane;
        pa.tv = (unsigned)(((i & 7) * 80 + min(v >> 1, 79)) * 3072 + cb * 192 + (v & 1) * 188); pa.tb = (const char*)w1;
      }
      pa.tl = lds0 + P1_TCH;
    }
    {
      const int q4 = (lane & 15) >> 2;
      const lds_cptr wb = (lds_cptr)lds + P1_W + (8 * hh + q4) * 256 + (nt ^ q4) * 64 + ((lane >> 4) & 1) * 32 + (lane & 3) * 8;
#pragma unroll
      for (int j = 0; j < 4; ++j) {
        const int ks = 2 * ((j >> 1) ^ wh) + ((j & 1) ^ tp);
        const int cs = ((2 * ks + hh) ^ ((r >> 1) & 7)) << 4;
        pa.x0[j] = (lds_cptr)lds + (tk * 32 + r) * 128 + cs;
        pa.x1[j] = (lds_cptr)lds + (to * 32 + r) * 128 + cs;
        pa.w[j] = wb + ks * 4096;
      }
      pa.csw = (LDS_AS float*)(LDS_AS char*)&lds[P1_CSA] + w * 64 + lane;
      pa.ws = ws; pa.g0 = WS_RS + (p * 128 + tk * 32 + r) * 16; pa.g1 = WS_RS + (p * 128 + to * 32 + r) * 16;
    }
    lds_cptr wpb[2][3];
#pragma unroll
    for (int j = 0; j < 2; ++j) {
      wpb[j][0] = (lds_cptr)lds + (32 * wh + 16 * (j ^ tp) + 8 * hh + ((lane & 15) >> 2)) * 192 + (((lane >> 4) & 1) ^ wh) * 32 + (lane & 3) * 8;
      wpb[j][1] = wpb[j][0] + 65536; wpb[j][2] = wpb[j][0] + 131072;
    }
    bf16x8 xk[20];
    FragB g0, g1;
    StatA st;
    st.lo = 0.f; st.hi = 0.f;
    {
      f32x16 acc[2] = {};
      if (cb >= 10) p1_pass_a<true>(acc, xk, pa, d, st);
      else p1_pass_a<false>(acc, xk, pa, d, st);
      STAMP(3);
      const float csp = ((LDS_AS float*)(LDS_AS char*)&lds[P1_CSA])[(w ^ 1) * 64 + lane];
      rd_b<0, 0, 0>(g0, wpb); rd_b<0, 0, 1>(g0, wpb); rd_b<0, 0, 2>(g0, wpb); rd_b<0, 1, 0>(g0, wpb); rd_b<0, 1, 1>(g0, wpb); rd_b<0, 1, 2>(g0, wpb);
      SB();
      if (__builtin_expect(!__all(st.gk[3] == tag && st.go[3] == tag), 0)) {
#pragma nounroll
        for (unsigned sp = 0; sp < SPIN_CAP; ++sp) {
          st.gk = __builtin_bit_cast(u32x4, LD16(pa.g0, 0)); st.go = __builtin_bit_cast(u32x4, LD16(pa.g1, 0));
          if (__all(st.gk[3] == tag && st.go[3] == tag)) break;
          __builtin_amdgcn_s_sleep(3);
          asm volatile("" ::: "memory");
        }
      }
      const bf16x8 csf = split_a(tp ? csp + st.lo : st.lo + csp);
      const bf16x8 mk = split_b(-__uint_as_float(st.gk[0])), mo = split_b(-__uint_as_float(st.go[0]));
      if (cb >= 10) {
        acc[0] = MFMA32(mk, csf, acc[0]); acc[1] = MFMA32(mo, csf, acc[1]);
        LDS_AS float* rsv = (LDS_AS float*)(LDS_AS char*)&lds[P1_RSV] + w * 64;
        rsv[lane] = __uint_as_float(hh ? st.go[1] : st.gk[1]);
        const int n = (qt0 + nt) * 32 + r - 1280;
        const int hd = n / 20, dd = n - hd * 20;
        const unsigned ov = dd == 18 ? 0x3f803f80u : 0u;
#pragma unroll
        for (int t = 0; t < 2; ++t) {
          unsigned pk[8];
#pragma unroll
          for (int g = 0; g < 4; ++g) {
            const f32x4 rr = *(const LDS_AS f32x4*)(rsv + t * 32 + 8 * g + 4 * hh);
            pk[2 * g] = cvtpk(acc[t][4 * g] * rr[0], acc[t][4 * g + 1] * rr[1]); pk[2 * g + 1] = cvtpk(acc[t][4 * g + 2] * rr[2], acc[t][4 * g + 3] * rr[3]);
          }
          const unsigned vp = nt < nq ? WS_KV + hd * 196608 + (p * 4 + (t ? to : tk)) * 3072 + 1536 + (hh * 24 + dd) * 16 : OOB;
          ST16(((u32x4){pk[0], pk[1], pk[2], pk[3]}), vp);
          ST16(((u32x4){pk[4], pk[5], pk[6], pk[7]}), vp + 768);
          const unsigned vq = dd >= 18 ? vp + 32 : OOB;
          ST16(((u32x4){ov, ov, ov, ov}), vq);
          ST16(((u32x4){ov, ov, ov, ov}), vq + 768);
        }
      } else {
        acc[0] = MFMA32(csf, mk, acc[0]); acc[1] = MFMA32(csf, mo, acc[1]);
        const bool isq = cb < 5;
        const float qs = isq ? QSCALE : 1.0f;
        const float rs2[2] = {qs * __uint_as_float(st.gk[1]), qs * __uint_as_float(st.go[1])};
        const unsigned ts = isq ? 1536u : 3072u;
        const unsigned base = (isq ? WS_Q : WS_KV) + p * 4 * ts + r * 16;
        const unsigned hs = isq ? 64u * 1536u : 196608u;
        const unsigned cst = isq ? 0u : 0x3f80u;
#pragma unroll
        for (int g = 0; g < 4; g += 2) {
          const unsigned os = (qt0 + nt) * 4 + g + hh - (isq ? 0 : 80);
          const unsigned hp = os / 5u, oi = os - hp * 5u;
          const bool sp = oi == 2;
          const unsigned a0 = base + (2 * hp + (oi > 2)) * hs + (oi > 2 ? oi - 3 : oi) * 512;
#pragma unroll
          for (int t = 0; t < 2; ++t) {
            const float rs = rs2[t];
            const auto s0 = __builtin_amdgcn_permlane32_swap(cvtpk(acc[t][4 * g] * rs, acc[t][4 * g + 1] * rs),
                                                             cvtpk(acc[t][4 * g + 4] * rs, acc[t][4 * g + 5] * rs), false, false);
            const auto s1 = __builtin_amdgcn_permlane32_swap(cvtpk(acc[t][4 * g + 2] * rs, acc[t][4 * g + 3] * rs),
                                                             cvtpk(acc[t][4 * g + 6] * rs, acc[t][4 * g + 7] * rs), false, false);
            const unsigned a1 = a0 + (t ? to : tk) * ts;
            ST16(((u32x4){s0[0], s1[0], sp ? cst : s0[1], sp ? 0u : s1[1]}), a1);
            ST16(((u32x4){s0[1], s1[1], cst, 0u}), sp ? a1 + hs : OOB);
          }
        }
      }
      SB();
    }
    STAMP(16);
    const float mean_k = __uint_as_float(st.gk[0]), rs = __uint_as_float(st.gk[2]);
    f32x16 acc[3] = {};
    float cbs[6] = {};
    const int par = (nt & 1) ^ wh;
    p1_stage_b<0>(acc, g1, g0, xk, wpb, cbs, par);
    vm_bar<14>();
    const int bk = w * 256 + b;
    const int rto = w == 0 ? 24576 : w == 1 ? 57344 : 65536;
    if (bk < 544) {
      const int kk = bk % 68, c8 = bk / 68;
      const int r0 = kk < 48 ? 32 * kk : kk < 64 ? 40 * (kk - 48) : 160 * (kk - 64) + 16;
      const int ra = kk < 48 ? 16 : kk < 64 ? 20 : 80, rbb = kk < 64 ? 8 : 20, rc = kk < 64 ? 4 : 40;
#pragma unroll
      for (int q = 0; q < 5; ++q) {
        const int u = q * 64 + lane, rho = u / 10;
        const int row = r0 + ra * (rho >> 4) + rc * ((rho >> 3) & 1) + rbb * ((rho >> 2) & 1) + (rho & 3);
        glds16((unsigned)(row * 1280 + c8 * 160 + (u % 10) * 16), kk < 48 ? w2 : wo, lds0 + rto + q * 1024);
      }
    }
    p1_stage_b<1>(acc, g0, g1, xk, wpb, cbs, par); p1_stage_b<2>(acc, g1, g0, xk, wpb, cbs, par);
    if (bk < 544) vm_bar<16>(); else vm_bar<11>();
    STAMP(17);
    p1_stage_b<3>(acc, g0, g1, xk, wpb, cbs, par); p1_stage_b<4>(acc, g1, g0, xk, wpb, cbs, par);
    STAMP(18);
    if (bk < 544) vm_bar<5>(); else vm_bar<0>();
    STAMP(19);
    if (tid == 0) st_flag(ws, WS_F1 + id1 * 128, epoch);
    p1_stage_b<5>(acc, g0, g1, xk, wpb, cbs, par); p1_stage_b<6>(acc, g1, g0, xk, wpb, cbs, par);
    p1_stage_b<7>(acc, g0, g1, xk, wpb, cbs, par);
    if (bk < 544) {
      wait_vm<0>();
      const lds_cptr rp = (lds_cptr)lds + rto + (8 * (lane >> 4) + ((lane & 15) >> 2)) * 160 + (lane & 3) * 8;
#pragma unroll
      for (int t = 0; t < 5; ++t)
        ST16(__builtin_bit_cast(u32x4, cat8(lds_tr(rp + t * 32), lds_tr(rp + t * 32 + 640))), WS_WT + (bk * 5 + t) * 1024 + lane * 16);
    }
    unsigned fo1 = 0, fo2 = 0;
    u64 fa = 0, fb = 0;
    if (w == 3) {
      const int ca = (head * 20) >> 7, cbb = (head * 20 + 19) >> 7;
      const int va = v_colblock(40 + ((head * 20) >> 5)), vb = v_colblock(40 + ((head * 20 + 19) >> 5));
      const int sel = lane >> 4;
      const int c1 = sel == 0 ? 5 + ca : sel == 1 ? 5 + cbb : sel == 2 ? va : vb;
      fo1 = WS_F1 + ((lane & 15) * 16 + c1) * 128;
      fo2 = WS_F1 + ((2 * qb + (lane & 1)) * 16 + ((lane & 2) ? cbb : ca)) * 128;
      fa = ld_flag(ws, fo1); fb = ld_flag(ws, fo2);
    }
    p1_stage_b<8>(acc, g1, g0, xk, wpb, cbs, par);
    p1_stage_b<9>(acc, g0, g1, xk, wpb, cbs, par);
    STAMP(4);
    LDS_AS f32x4* xch = (LDS_AS f32x4*)(LDS_AS char*)&lds[73728];
    LDS_AS float* cst = (LDS_AS float*)(LDS_AS char*)&lds[73728 + 49152];
    const int rsw = lane ^ (wh << 4);
#pragma unroll
    for (int t = 0; t < 3; ++t) {
      cst[(t * 64 + rsw) * 8 + w] = cbs[2 * t] + cbs[2 * t + 1];
#pragma unroll
      for (int u = 0; u < 2; ++u)
        xch[(w * 6 + 2 * t + u) * 64 + lane] = (f32x4){acc[t][8 + 4 * u], acc[t][9 + 4 * u], acc[t][10 + 4 * u], acc[t][11 + 4 * u]};
    }
    if (w == 3) { SB(); if (!__all(fa == epoch && fb == epoch)) poll2(ws, fo1, fo2, epoch); SB(); }
    bar_lds();
    STAMP(20);
    f32x4 c0[3], c1[3], px[6];
    u32x2 bb[6];
#pragma unroll
    for (int t = 0; t < 3; ++t) { c0[t] = *(const LDS_AS f32x4*)(cst + (t * 64 + rsw) * 8); c1[t] = *(const LDS_AS f32x4*)(cst + (t * 64 + rsw) * 8 + 4); }
#pragma unroll
    for (int q = 0; q < 6; ++q) {
      px[q] = xch[((w ^ 4) * 6 + q) * 64 + lane];
      bb[q] = *(const u32x2*)(lds + P1_B1 + ((q >> 1) * 32 + 16 * wh + 8 * (q & 1) + 4 * hh) * 2);
    }
    SB();
    glds16x3(vdma, kvg, lds0 + w * 3072);
    q0 = LD16(qo + lane * 16, 0);
    q1u = __builtin_bit_cast(u32x4, LD16(qo + 1024 + r * 16, 0));
    SB();
    glds16x3(vdma, kvg + P2_STG, lds0 + P2_STG + w * 3072);
    glds16x3(vdma, kvg + 2 * P2_STG, lds0 + 2 * P2_STG + w * 3072);
    SB();
    const unsigned hp = WS_H + ((p * 4 + tk) * 96 + cb * 6 + wh) * 1024 + lane * 16;
    const bf16x8 mk = split_b(-mean_k);
#pragma unroll
    for (int t = 0; t < 3; ++t) {
      const f32x4 cc = c0[t] + c1[t];
      acc[t] = MFMA32(split_a((cc[0] + cc[1]) + (cc[2] + cc[3])), mk, acc[t]);
    }
#pragma unroll
    for (int t = 0; t < 3; ++t) {
      unsigned pk[4];
#pragma unroll
      for (int u = 0; u < 2; ++u) {
        const f32x4 a = (f32x4){acc[t][4 * u], acc[t][4 * u + 1], acc[t][4 * u + 2], acc[t][4 * u + 3]} + px[2 * t + u];
        const u32x2 bq = bb[2 * t + u];
        pk[2 * u] = gelu2((f32x2){a[0], a[1]} * rs + (f32x2){bflo(bq[0]), bfhi(bq[0])});
        pk[2 * u + 1] = gelu2((f32x2){a[2], a[3]} * rs + (f32x2){bflo(bq[1]), bfhi(bq[1])});
      }
      ST16(((u32x4){pk[0], pk[1], pk[2], pk[3]}), hp + t * 2048);
    }
    STAMP(21);
  }

  const int rb = xcd * 4 + (i & 3), cq = i >> 2;
  const unsigned ao = (lane >> 5) * 1024 + (((lane >> 4) & 1) * 32 + (lane & 15)) * 16;
  const unsigned wofs = WS_WT + cq * 68 * 5 * 1024 + lane * 16;
  bf16x8 af[3][4], wf[3][5];
  auto ldha = [&](int s, int kk) {
#pragma unroll
    for (int m = 0; m < 4; ++m) af[s][m] = LD16(ao + (m & 1) * 256, WS_H + ((2 * rb + (m >> 1)) * 96 + 2 * kk) * 1024);
  };
  auto ldw = [&](int s, int kk) {
#pragma unroll
    for (int t = 0; t < 5; ++t) wf[s][t] = LD16(wofs + t * 1024, kk * 5120);
  };
  auto ldh = [&](int s, int kk) { ldha(s, kk); ldw(s, kk); };
  const int kh0 = w < 4 ? 9 * w : 36 + 3 * (w - 4);
  auto wo_piece = [&](int q) {
    const int n = min(w * 8 + q, 59), j = n / 15;
    glds16(wofs, ws + (48 + 5 * j) * 5120 + (n - 15 * j) * 1024, lds0 + P3_WO + n * 1024);
  };
  {
    if (hh) q1u = (u32x4){0u, 0u, 0u, 0u};
    const int dcl = r < 21 ? r : 21;
    const int k1off = hh ? 1536 + 21 * 16 - lane * 16 : 1024;
    const lds_cptr kb = (lds_cptr)lds + lane * 16;
    const lds_cptr vb = (lds_cptr)lds + 1536 + (hh * 24 + dcl) * 16;
    const f32x16 zero = {};
    wait_vm_bar<6>();
    STAMP(22);
    bf16x8 k0 = lds_b128(kb), k1 = lds_b128(kb + k1off);
    f32x16 sa = MFMA32(k0, q0, zero), sb;
    {
      f32x16 s = MFMA32(k1, __builtin_bit_cast(bf16x8, q1u), sa);
      float m = s[0];
#pragma unroll
      for (int q = 1; q < 16; ++q) m = fmaxf(m, s[q]);
      m = xhalf_max(m);
      q1u[2] = hh ? 0u : (cvtpk(-m, 0.0f) & 0xffffu);
    }
    const bf16x8 q1 = __builtin_bit_cast(bf16x8, q1u);
    sa = MFMA32(k1, q1, sa);
    k0 = lds_b128(kb + 3072); k1 = lds_b128(kb + 3072 + k1off);
    bf16x8 v0 = lds_b128(vb), v1 = {};
    unsigned pp[8] = {};
    float e[4] = {};
    f32x16 o = {};
    int sc = 0;
    u64 fl = 0;
    const unsigned flo = WS_F1B + (w * 32 + r) * 128;
    __builtin_amdgcn_s_setprio(2);
    STAMP(6);
#define ATTN_STAGE(HOOK) do { \
      const int sn = sc == 2 * P2_STG ? 0 : sc + P2_STG; \
      const lds_cptr kc = kb + sc, kn = kb + sn, vc = vb + sc, vn = vb + sn; \
      attn_step<0>(HOOK, sa, sb, o, q0, q1, k0, k1, v0, v1, pp, e, kc, kn, vc, vn, k1off); \
      attn_step<1>(HOOK, sb, sa, o, q0, q1, k0, k1, v0, v1, pp, e, kc, kn, vc, vn, k1off); \
      attn_step<2>(HOOK, sa, sb, o, q0, q1, k0, k1, v0, v1, pp, e, kc, kn, vc, vn, k1off); \
      attn_step<3>(HOOK, sb, sa, o, q0, q1, k0, k1, v0, v1, pp, e, kc, kn, vc, vn, k1off); \
      attn_step<4>(HOOK, sa, sb, o, q0, q1, k0, k1, v0, v1, pp, e, kc, kn, vc, vn, k1off); \
      attn_step<5>(HOOK, sb, sa, o, q0, q1, k0, k1, v0, v1, pp, e, kc, kn, vc, vn, k1off); \
      attn_step<6>(HOOK, sa, sb, o, q0, q1, k0, k1, v0, v1, pp, e, kc, kn, vc, vn, k1off); \
      attn_step<7>(HOOK, sb, sa, o, q0, q1, k0, k1, v0, v1, pp, e, kc, kn, vc, vn, k1off); \
      scp = sc; sc = sn; } while (0)
    int scp;
#pragma nounroll
    for (int st = 0; st < 6; ++st) {
      ATTN_STAGE([](int) {});
      if (st == 5 && !__all(fl == epoch)) poll2(ws, flo, flo, epoch);
      asm volatile("s_waitcnt vmcnt(0) lgkmcnt(2)\n\ts_barrier" ::: "memory");
      if (st < 5) glds16x3(vdma, kvg + (st + 3) * P2_STG, lds0 + scp + w * 3072);
      if (st == 4) fl = ld_flag(ws, flo);
      if (st == 0 && tid == 0) st_flag(ws, WS_F1B + b * 128, epoch);
      SB();
    }
    auto pf = [&](int n) {
      if (n < 27) {
        const int s = n / 9, j = n % 9, kk = kh0 + s;
        if (j < 4) af[s][j] = LD16(ao + (j & 1) * 256, WS_H + ((2 * rb + (j >> 1)) * 96 + 2 * kk) * 1024);
        else wf[s][j - 4] = LD16(wofs + (j - 4) * 1024, kk * 5120);
      } else {
        wo_piece(n - 24);
      }
    };
    wo_piece(0); wo_piece(1); wo_piece(2);
    ATTN_STAGE([&](int n) { pf(n); });
    ATTN_STAGE([&](int n) { pf(n + 16); });
    STAMP(7);
    pp[6] = cvtpk(e[0], e[1]); pp[7] = cvtpk(e[2], e[3]);
    o = MFMA32(v1, PK4(pp[4], pp[5], pp[6], pp[7]), o);
    float l = upper_half(o[8]);
    float amax = 0.f;
#pragma unroll
    for (int q = 0; q < 12; ++q) amax = fmaxf(amax, fabsf(o[q]));
    const bool bad = !(l < 1.0e9f) || !(l > 0.25f) || !(amax < 1.0e30f);
    if (__builtin_expect(__any(bad), 0)) {
      q1u[2] = 0u;
      const bf16x8 q1z = __builtin_bit_cast(bf16x8, q1u);
      float m = -INFINITY;
      o = zero;
#pragma nounroll
      for (int t = 0; t < 64; ++t) {
        const unsigned tp = WS_KV + head * 196608 + t * 3072;
        f32x16 s = MFMA32(LD16(tp + lane * 16, 0), q0, zero);
        s = MFMA32(LD16(tp + k1off + lane * 16, 0), q1z, s);
        float tm = s[0];
#pragma unroll
        for (int q = 1; q < 16; ++q) tm = fmaxf(tm, s[q]);
        tm = xhalf_max(tm);
        const float mn = fmaxf(m, tm);
        const float al = EXP2(m - mn);
        m = mn;
        unsigned pk[8];
#pragma unroll
        for (int q = 0; q < 8; ++q) pk[q] = cvtpk(EXP2(s[2 * q] - mn), EXP2(s[2 * q + 1] - mn));
#pragma unroll
        for (int q = 0; q < 16; ++q) o[q] *= al;
        const unsigned vp = tp + 1536 + (hh * 24 + dcl) * 16;
        o = MFMA32(LD16(vp, 0), PK4(pk[0], pk[1], pk[2], pk[3]), o);
        o = MFMA32(LD16(vp + 768, 0), PK4(pk[4], pk[5], pk[6], pk[7]), o);
      }
      l = upper_half(o[8]);
    }
    const float il = 1.0f / l;
    const unsigned op = WS_O + (qb * 8 + w) * 40 * 1024;
    ST16(((u32x4){cvtpk(o[0] * il, o[1] * il), cvtpk(o[2] * il, o[3] * il), cvtpk(o[4] * il, o[5] * il), cvtpk(o[6] * il, o[7] * il)}),
         op + head * 1024 + lane * 16);
    ST8(((u32x2){cvtpk(o[8] * il, o[9] * il), cvtpk(o[10] * il, o[11] * il)}),
        hh ? OOB : op + (32 + (head >> 2)) * 1024 + (((head >> 1) & 1) * 32 + r) * 16 + (head & 1) * 8);
    STAMP(8);
  }

  {
    const int g = lane >> 4, c = lane & 15;
    f32x4 acc[4][5] = {};
    LDS_AS f32x4* red = (LDS_AS f32x4*)(LDS_AS char*)lds;
    LDS_AS unsigned* cnt = (LDS_AS unsigned*)(LDS_AS char*)&lds[P1_CNT];
    auto mm = [&](int s) {
      SB();
#pragma unroll
      for (int m = 0; m < 4; ++m)
#pragma unroll
        for (int t = 0; t < 5; ++t) acc[m][t] = MFMA16(wf[s][t], af[s][m], acc[m][t]);
      SB();
    };
    wait_vm<0>();
    {
      unsigned old = 0;
      if (lane == 0) old = __hip_atomic_fetch_add(cnt, 1u, __ATOMIC_RELAXED, __HIP_MEMORY_SCOPE_WORKGROUP);
      if (lane == 0 && old == 7) {
        st_flag(ws, WS_F2 + (qb * 32 + head) * 128, epoch);
        if (b == 0) st_flag(ws, WS_SYNC, epoch);
      }
    }
    STAMP(13);
    __builtin_amdgcn_s_setprio(0);
    mm(0);
    u32x2 xv[3] = {}, bv[3] = {};
    auto ldres = [&]() {
#pragma unroll
      for (int u = 0; u < 3; ++u) {
        const int tl = min(w + 8 * u, 19), m = tl / 5, t = tl - m * 5;
        xv[u] = *(const u32x2*)(x + (size_t)(rb * 64 + m * 16 + c) * 640 + cq * 80 + t * 16 + 4 * g);
        bv[u] = *(const u32x2*)(b2 + cq * 80 + t * 16 + 4 * g);
      }
    };
    const int j = w & 3;
    ldw(0, w < 4 ? kh0 + 3 : 48 + 5 * j + 3);
    if (w < 4) ldha(0, kh0 + 3);
    mm(1);
    ldw(1, w < 4 ? kh0 + 4 : 48 + 5 * j + 4);
    if (w < 4) ldha(1, kh0 + 4);
    mm(2);
    if (w < 4) {
      ldh(2, kh0 + 5);
      mm(0); ldh(0, kh0 + 6);
      mm(1); ldh(1, kh0 + 7);
      mm(2); ldh(2, kh0 + 8);
      mm(0); ldres();
      mm(1); mm(2);
      STAMP(14);
      lds_wait(lds0 + P1_CNT, 8u);
#pragma unroll
      for (int m = 0; m < 4; ++m)
#pragma unroll
        for (int t = 0; t < 5; ++t) red[(w * 20 + m * 5 + t) * 64 + lane] = acc[m][t];
      asm volatile("s_waitcnt lgkmcnt(0)" ::: "memory");
      __hip_atomic_store(cnt + 4 + w, 1u, __ATOMIC_RELAXED, __HIP_MEMORY_SCOPE_WORKGROUP);
      STAMP(10);
    }
    int w2 = w;
    asm volatile("" : "+s"(w2));
    if (w2 >= 4) {
      STAMP(14);
      STAMP(15);
      {
        const unsigned fo = WS_F2 + ((rb >> 2) * 32 + (lane & 31)) * 128;
        poll2(ws, fo, fo, epoch);
      }
      STAMP(9);
      bf16x8 of[5][4];
#pragma unroll
      for (int k = 0; k < 5; ++k)
#pragma unroll
        for (int m = 0; m < 4; ++m) of[k][m] = LD16(ao + (m & 1) * 256, WS_O + ((2 * rb + (m >> 1)) * 40 + 2 * (5 * j + (k + 3) % 5)) * 1024);
      SB();
      lds_wait(lds0 + P1_CNT, 8u);
      const lds_cptr wol = (lds_cptr)lds + P3_WO + j * 15360 + lane * 16;
#pragma unroll
      for (int k = 0; k < 5; ++k) {
        if (k >= 1 && k < 4) {
#pragma unroll
          for (int t = 0; t < 5; ++t) wf[(k + 1) & 1][t] = lds_b128(wol + ((k - 1) * 5 + t) * 1024);
        }
        if (k == 2) ldres();
        SB();
#pragma unroll
        for (int m = 0; m < 4; ++m)
#pragma unroll
          for (int t = 0; t < 5; ++t) acc[m][t] = MFMA16(wf[k & 1][t], of[k][m], acc[m][t]);
        SB();
      }
      STAMP(10);
      lds_wait(lds0 + P1_CNT + 16 + 4 * j, 1u);
#pragma unroll
      for (int m = 0; m < 4; ++m) {
#pragma unroll
        for (int t = 0; t < 5; ++t) red[(j * 20 + m * 5 + t) * 64 + lane] += acc[m][t];
      }
    }
    bar_lds();
    STAMP(11);
#pragma unroll
    for (int u = 0; u < 3; ++u) {
      const int tl = w + 8 * u;
      if (tl < 20) {
        const int m = tl / 5, t = tl - m * 5;
        f32x4 a = red[tl * 64 + lane];
#pragma unroll
        for (int q = 1; q < 4; ++q) a += red[(q * 20 + tl) * 64 + lane];
        *(u32x2*)(out + (size_t)(rb * 64 + m * 16 + c) * 640 + cq * 80 + t * 16 + 4 * g) =
            (u32x2){cvtpk(a[0] + bflo(xv[u][0]) + bflo(bv[u][0]), a[1] + bfhi(xv[u][0]) + bfhi(bv[u][0])),
                    cvtpk(a[2] + bflo(xv[u][1]) + bflo(bv[u][1]), a[3] + bfhi(xv[u][1]) + bfhi(bv[u][1]))};
      }
    }
    STAMP(12);
  }
}

extern "C" void kernel_launch(void* const* d_in, const int* in_sizes, int n_in, void* d_out, int out_size, void* d_ws, size_t ws_size,
                              hipStream_t stream) {
  mega_kernel<<<256, 512, 0, stream>>>((const u16*)d_in[0], (const u16*)d_in[1], (const u16*)d_in[2], (const u16*)d_in[4], (const u16*)d_in[5],
                                       (const u16*)d_in[6], (const u16*)d_in[7], (char*)d_ws, (u16*)d_out);
}
```

```cpp
#define PFC 53888
#include <hip/hip_runtime.h>
#include <hip/hip_bf16.h>
#include <stdint.h>

typedef unsigned short u16;
typedef unsigned long long u64;
typedef short bf16x8 __attribute__((ext_vector_type(8)));
typedef short s16x4 __attribute__((ext_vector_type(4)));
typedef float f32x16 __attribute__((ext_vector_type(16)));
typedef float f32x4 __attribute__((ext_vector_type(4)));
typedef float f32x2 __attribute__((ext_vector_type(2)));
typedef unsigned u32x4 __attribute__((ext_vector_type(4)));
typedef unsigned u32x2 __attribute__((ext_vector_type(2)));
typedef __bf16 bf2 __attribute__((ext_vector_type(2)));
#define LDS_AS __attribute__((address_space(3)))
#define GAS __attribute__((address_space(1)))
typedef LDS_AS const char* lds_cptr;
typedef GAS u64 gu64;
#define RLX_AGENT __ATOMIC_RELAXED, __HIP_MEMORY_SCOPE_AGENT

#define PFW "4"
constexpr float EPS = 1e-5f;
constexpr float QSCALE = 0.22360679774997896f * 1.4426950408889634f;

constexpr unsigned WS_SYNC = 0;
constexpr unsigned WS_F0 = 4096, WS_F1 = WS_F0 + 32768, WS_F1B = WS_F1 + 32768, WS_F2 = WS_F1B + 32768;
constexpr unsigned WS_Q = WS_F2 + 32768;
constexpr unsigned WS_KV = WS_Q + 32 * 64 * 1536;
constexpr unsigned WS_O = WS_KV + 32 * 196608;
constexpr unsigned WS_H = WS_O + 64 * 40 * 1024;
constexpr unsigned WS_XN = WS_H + 64 * 96 * 1024;
constexpr unsigned WS_RS = WS_XN;
constexpr unsigned WS_WT = WS_RS + 32768;
constexpr unsigned WS_END = WS_WT + 8 * 68 * 5 * 1024;

#define MFMA32(a, b, c) __builtin_amdgcn_mfma_f32_32x32x16_bf16(a, b, c, 0, 0, 0)
#define MFMA16(a, b, c) __builtin_amdgcn_mfma_f32_16x16x32_bf16(a, b, c, 0, 0, 0)
#define SB() __builtin_amdgcn_sched_barrier(0)
#define EXP2(v) __builtin_amdgcn_exp2f(v)
#define PK4(a, b, c, d) __builtin_bit_cast(bf16x8, ((u32x4){a, b, c, d}))
#define ST16(v, off) __builtin_amdgcn_raw_buffer_store_b128(v, wsr, off, 0, 16)
#define ST8(v, off) __builtin_amdgcn_raw_buffer_store_b64(v, wsr, off, 0, 16)
#define LD16(off, soff) __builtin_bit_cast(bf16x8, __builtin_amdgcn_raw_buffer_load_b128(wsr, off, soff, 16))

#define STAMP(i)

__device__ __forceinline__ unsigned cvtpk(float lo, float hi) {
  return __builtin_bit_cast(unsigned, __builtin_convertvector((f32x2){lo, hi}, bf2));
}
__device__ __forceinline__ float bflo(unsigned u) { return __uint_as_float(u << 16); }
__device__ __forceinline__ float bfhi(unsigned u) { return __uint_as_float(u & 0xffff0000u); }
__device__ __forceinline__ void glds4(unsigned voff, const void* sbase, unsigned ldsb) {
  unsigned sv;
  asm volatile("s_mov_b32 %0, m0\n\ts_mov_b32 m0, %3\n\ts_nop 0\n\tglobal_load_lds_dword %1, %2 sc1\n\ts_mov_b32 m0, %0"
               : "=&s"(sv) : "v"(voff), "s"(sbase), "s"(ldsb) : "memory");
}
__device__ __forceinline__ void glds16x3(unsigned voff, const void* sbase, unsigned ldsb) {
  unsigned sv;
  asm volatile("s_mov_b32 %0, m0\n\ts_mov_b32 m0, %3\n\ts_nop 0\n\t"
               "global_load_lds_dwordx4 %1, %2 sc1\n\t"
               "global_load_lds_dwordx4 %1, %2 offset:1024 sc1\n\t"
               "global_load_lds_dwordx4 %1, %2 offset:2048 sc1\n\t"
               "s_mov_b32 m0, %0"
               : "=&s"(sv) : "v"(voff), "s"(sbase), "s"(ldsb) : "memory");
}
__device__ __forceinline__ void glds16x4(unsigned voff, const void* sbase, unsigned ldsb) {
  unsigned sv;
  asm volatile("s_mov_b32 %0, m0\n\ts_mov_b32 m0, %3\n\ts_nop 0\n\t"
               "global_load_lds_dwordx4 %1, %2 sc1\n\t"
               "global_load_lds_dwordx4 %1, %2 offset:1024 sc1\n\t"
               "global_load_lds_dwordx4 %1, %2 offset:2048 sc1\n\t"
               "global_load_lds_dwordx4 %1, %2 offset:3072 sc1\n\t"
               "s_mov_b32 m0, %0"
               : "=&s"(sv) : "v"(voff), "s"(sbase), "s"(ldsb) : "memory");
}
template <int N> __device__ __forceinline__ void wait_vm() { asm volatile("s_waitcnt vmcnt(%0)" ::"i"(N) : "memory"); }
template <int N> __device__ __forceinline__ void wait_vm_bar() {
  __builtin_amdgcn_s_waitcnt(0xC07F);
  asm volatile("s_waitcnt vmcnt(%0) lgkmcnt(0)\n\ts_barrier" ::"i"(N) : "memory");
}
__device__ __forceinline__ void bar_lds() { asm volatile("s_waitcnt lgkmcnt(0)\n\ts_barrier" ::: "memory"); }
__device__ __forceinline__ bf16x8 lds_b128(lds_cptr p) { return *(const LDS_AS bf16x8*)p; }
__device__ __forceinline__ s16x4 lds_tr(lds_cptr p) { return __builtin_amdgcn_ds_read_tr16_b64_v4i16((LDS_AS s16x4*)p); }
__device__ __forceinline__ bf16x8 cat8(s16x4 a, s16x4 b) { return __builtin_shufflevector(a, b, 0, 1, 2, 3, 4, 5, 6, 7); }
__device__ __forceinline__ float xhalf_max(float v) {
  auto rr = __builtin_amdgcn_permlane32_swap(__float_as_uint(v), __float_as_uint(v), false, false);
  return fmaxf(__uint_as_float(rr[0]), __uint_as_float(rr[1]));
}
__device__ __forceinline__ float upper_half(float v) {
  auto rr = __builtin_amdgcn_permlane32_swap(__float_as_uint(v), __float_as_uint(v), false, false);
  return __uint_as_float(rr[1]);
}
__device__ __forceinline__ float gelu_tanh(float x) {
  const float c0 = -2.0f * 0.7978845608028654f * 1.4426950408889634f;
  const float c1 = c0 * 0.044715f;
  float t = fmaf(x * x, c1, c0);
  float e = __builtin_amdgcn_exp2f(x * t);
  return x * __builtin_amdgcn_rcpf(1.0f + e);
}
__device__ __forceinline__ unsigned gelu2(f32x2 x) {
  const float c0 = -2.0f * 0.7978845608028654f * 1.4426950408889634f;
  const f32x2 k0 = {c0, c0}, k1 = {c0 * 0.044715f, c0 * 0.044715f}, one = {1.0f, 1.0f};
  const f32x2 z = x * __builtin_elementwise_fma(x * x, k1, k0);
  const f32x2 d = (f32x2){__builtin_amdgcn_exp2f(z[0]), __builtin_amdgcn_exp2f(z[1])} + one;
  const f32x2 g = x * (f32x2){__builtin_amdgcn_rcpf(d[0]), __builtin_amdgcn_rcpf(d[1])};
  return cvtpk(g[0], g[1]);
}
template <int CTRL, int RMASK> __device__ __forceinline__ float dpp_add(float v) {
  return v + __uint_as_float(__builtin_amdgcn_update_dpp(0u, __float_as_uint(v), CTRL, RMASK, 0xf, false));
}
__device__ __forceinline__ float wave_sum(float v) {
  v = dpp_add<0x111, 0xf>(v); v = dpp_add<0x112, 0xf>(v); v = dpp_add<0x114, 0xf>(v); v = dpp_add<0x118, 0xf>(v);
  v = dpp_add<0x142, 0xa>(v); v = dpp_add<0x143, 0xc>(v);
  return __uint_as_float(__builtin_amdgcn_readlane(__float_as_uint(v), 63));
}
__device__ __forceinline__ u64 ld_flag(const char* ws, unsigned off) { return __hip_atomic_load((gu64*)(ws + off), RLX_AGENT); }
__device__ __forceinline__ void st_flag(char* ws, unsigned off, u64 v) { __hip_atomic_store((gu64*)(ws + off), v, RLX_AGENT); }
__device__ __forceinline__ u64 rfl64(u64 v) {
  return ((u64)(unsigned)__builtin_amdgcn_readfirstlane((unsigned)(v >> 32)) << 32) | (unsigned)__builtin_amdgcn_readfirstlane((unsigned)v);
}
constexpr unsigned SPIN_CAP = 1u << 18;
__device__ __forceinline__ void poll2(const char* ws, unsigned off1, unsigned off2, u64 epoch) {
  u64 a, b, m; unsigned c;
  asm volatile("s_mov_b32 %3, %8\n"
               ".Lpl%=:\n\t"
               "global_load_dwordx2 %0, %4, %6 sc1\n\t"
               "global_load_dwordx2 %1, %5, %6 sc1\n\t"
               "s_waitcnt vmcnt(0)\n\t"
               "v_cmp_ne_u64 %2, %7, %0\n\t"
               "v_cmp_ne_u64 vcc, %7, %1\n\t"
               "s_nop 1\n\t"
               "s_or_b64 %2, %2, vcc\n\t"
               "s_cbranch_scc0 .Lpd%=\n\t"
               "s_sleep 3\n\t"
               "s_sub_u32 %3, %3, 1\n\t"
               "s_cbranch_scc0 .Lpl%=\n"
               ".Lpd%=:"
               : "=&v"(a), "=&v"(b), "=&s"(m), "=&s"(c) : "v"(off1), "v"(off2), "s"(ws), "s"(epoch), "i"(SPIN_CAP) : "memory", "vcc", "scc");
}
__device__ __forceinline__ void lds_wait(unsigned la, unsigned want) {
  unsigned v, sv, c;
  asm volatile("s_mov_b32 %2, %5\n"
               ".Lwl%=:\n\t"
               "ds_read_b32 %0, %3\n\t"
               "s_waitcnt lgkmcnt(0)\n\t"
               "v_readfirstlane_b32 %1, %0\n\t"
               "s_nop 1\n\t"
               "s_cmp_ge_u32 %1, %4\n\t"
               "s_cbranch_scc1 .Lwd%=\n\t"
               "s_sleep 2\n\t"
               "s_sub_u32 %2, %2, 1\n\t"
               "s_cbranch_scc0 .Lwl%=\n"
               ".Lwd%=:"
               : "=&v"(v), "=&s"(sv), "=&s"(c) : "v"(la), "s"(want), "i"(SPIN_CAP * 8) : "memory", "scc");
}

constexpr int P1_SLOT = 32768, P1_W = 16384;
constexpr int P1_TOP = 4 * P1_SLOT;
constexpr int P1_B1 = P1_TOP + 24576, P1_CNT = P1_B1 + 256, P1_TCH = P1_CNT + 256, P1_CSA = P1_TCH + 256, P1_RSV = P1_CSA + 2048;
constexpr int LDS_BYTES = 163840;
static_assert(P1_RSV + 2048 <= LDS_BYTES, "lds");
constexpr unsigned OOB = 0x7ffffff0u;

__device__ __forceinline__ void glds16(unsigned voff, const void* sbase, unsigned ldsb) {
  unsigned sv;
  asm volatile("s_mov_b32 %0, m0\n\ts_mov_b32 m0, %3\n\ts_nop 0\n\tglobal_load_lds_dwordx4 %1, %2 sc1\n\ts_mov_b32 m0, %0"
               : "=&s"(sv) : "v"(voff), "s"(sbase), "s"(ldsb) : "memory");
}
__device__ __forceinline__ float addf(float a, float b) { float r; asm("v_add_f32_e32 %0, %1, %2" : "=v"(r) : "v"(a), "v"(b)); return r; }
__device__ __forceinline__ void colsum(float& lo, float& hi, const bf16x8& f) {
  const u32x4 u = __builtin_bit_cast(u32x4, f);
#pragma unroll
  for (int e = 0; e < 4; ++e) { lo = addf(lo, bflo(u[e])); hi = addf(hi, bfhi(u[e])); }
  asm volatile("" : "+v"(lo), "+v"(hi));
}
template <int E> __device__ __forceinline__ void colsum1(float& lo, float& hi, const bf16x8& f) {
  const unsigned u = __builtin_bit_cast(u32x4, f)[E];
  lo = addf(lo, bflo(u)); hi = addf(hi, bfhi(u));
  asm volatile("" : "+v"(lo), "+v"(hi));
}
__device__ __forceinline__ bf16x8 split_a(float v) {
  const unsigned h = cvtpk(v, 0.f) & 0xffffu, pk = h | (cvtpk(v - bflo(h), 0.f) << 16);
  return PK4(pk, pk, 0u, 0u);
}
__device__ __forceinline__ bf16x8 split_b(float v) {
  const unsigned h = cvtpk(v, v);
  const float l = v - bflo(h);
  return PK4(h, cvtpk(l, l), 0u, 0u);
}
struct P1Dma {
  unsigned vo[4], vm[3];
  const char *base, *wm;
  unsigned kstride;
  unsigned ldsw, lstep, ldsm;
};
template <int S, int J> __device__ __forceinline__ void p1_piece(const P1Dma& d) {
  if (S < 10) glds16(d.vo[J], d.base + S * d.kstride, d.ldsw + (S & 3) * P1_SLOT + J * d.lstep);
  else if (J < 3) glds16(d.vm[J < 3 ? J : 0], d.wm + 2 * (S - 10) * (64 * 3072), d.ldsm + (S == 14 ? P1_TOP : (S & 3) * P1_SLOT) + J * 4096);
}
template <int S> __device__ __forceinline__ void p1_issue(const P1Dma& d) { p1_piece<S, 0>(d); p1_piece<S, 1>(d); p1_piece<S, 2>(d); p1_piece<S, 3>(d); }

struct FragA { bf16x8 x0[2], x1[4], w[4]; };
struct PtrA { lds_cptr x0[4], x1[4], w[4]; LDS_AS float* csw; const char *ws, *tb; unsigned g0, g1, tv, tl; };
struct StatA { float lo, hi; u32x4 gk, go; };
template <int S, int J> __device__ __forceinline__ void rd_a(FragA& f, bf16x8 (&xk)[20], const PtrA& p) {
  constexpr int so = (S & 3) * P1_SLOT;
  if (J < 2) xk[2 * S + (J & 1)] = lds_b128(p.x0[J] + so); else f.x0[J & 1] = lds_b128(p.x0[J] + so);
  f.w[J] = cat8(lds_tr(p.w[J] + so), lds_tr(p.w[J] + so + 1024));
  f.x1[J] = lds_b128(p.x1[J] + so);
}
template <bool VT> __device__ __forceinline__ void mm_a(f32x16& acc, const bf16x8& xf, const bf16x8& wf) {
  SB(); acc = VT ? MFMA32(xf, wf, acc) : MFMA32(wf, xf, acc); SB();
}
template <bool VT, int S>
__device__ __forceinline__ void p1_stage_a(f32x16 (&acc)[2], FragA& fn, const FragA& fc, bf16x8 (&xk)[20], const PtrA& p, const P1Dma& d, StatA& st) {
  wait_vm_bar<(S == 0 ? 8 : S < 3 ? 9 : S == 3 ? 12 : S < 6 ? 11 : S == 6 ? 8 : S == 7 ? 9 : S == 8 ? 8 : 6)>();
  if (S == 9) asm volatile("" : "+v"(st.gk), "+v"(st.go));
#define CS(E, J) if (S < 9) colsum1<E>(st.lo, st.hi, fc.w[J])
  mm_a<VT>(acc[0], xk[2 * S], fc.w[0]);     if (S < 9) rd_a<(S + 1) % 10, 0>(fn, xk, p); CS(0, 0);
  mm_a<VT>(acc[1], fc.x1[0], fc.w[0]);      if (S < 9) rd_a<(S + 1) % 10, 2>(fn, xk, p); CS(1, 0);
  mm_a<VT>(acc[0], xk[2 * S + 1], fc.w[1]); p1_piece<S + 4, 0>(d); CS(2, 0);
  mm_a<VT>(acc[1], fc.x1[1], fc.w[1]);      if (S < 9) rd_a<(S + 1) % 10, 1>(fn, xk, p); CS(3, 0);
  mm_a<VT>(acc[0], fc.x0[0], fc.w[2]);      p1_piece<S + 4, 1>(d); CS(0, 2);
  mm_a<VT>(acc[1], fc.x1[2], fc.w[2]);      if (S < 9) rd_a<(S + 1) % 10, 3>(fn, xk, p); CS(1, 2);
  mm_a<VT>(acc[0], fc.x0[1], fc.w[3]);      p1_piece<S + 4, 2>(d); CS(2, 2);
  mm_a<VT>(acc[1], fc.x1[3], fc.w[3]);      p1_piece<S + 4, 3>(d); CS(3, 2);
#undef CS
  if (S == 0) glds4(p.tv, p.tb, p.tl);
  if (S == 2) p1_issue<14>(d);
  if (S == 6)
    asm volatile("global_load_dwordx4 %0, %2, %4 sc1\n\tglobal_load_dwordx4 %1, %3, %4 sc1" : "=&v"(st.gk), "=&v"(st.go) : "v"(p.g0), "v"(p.g1), "s"(p.ws) : "memory");
  if (S == 8) { colsum(st.lo, st.hi, fn.w[0]); colsum(st.lo, st.hi, fn.w[2]); st.lo += st.hi; *p.csw = st.lo; }
}
template <bool VT>
__device__ __forceinline__ void p1_pass_a(f32x16 (&acc)[2], bf16x8 (&xk)[20], const PtrA& p, const P1Dma& d, StatA& st) {
  FragA f0, f1;
  wait_vm_bar<13>();
  rd_a<0, 0>(f0, xk, p); rd_a<0, 2>(f0, xk, p); rd_a<0, 1>(f0, xk, p); rd_a<0, 3>(f0, xk, p);
  p1_stage_a<VT, 0>(acc, f1, f0, xk, p, d, st); p1_stage_a<VT, 1>(acc, f0, f1, xk, p, d, st);
  p1_stage_a<VT, 2>(acc, f1, f0, xk, p, d, st); p1_stage_a<VT, 3>(acc, f0, f1, xk, p, d, st);
  p1_stage_a<VT, 4>(acc, f1, f0, xk, p, d, st); p1_stage_a<VT, 5>(acc, f0, f1, xk, p, d, st);
  p1_stage_a<VT, 6>(acc, f1, f0, xk, p, d, st); p1_stage_a<VT, 7>(acc, f0, f1, xk, p, d, st);
  p1_stage_a<VT, 8>(acc, f1, f0, xk, p, d, st); p1_stage_a<VT, 9>(acc, f0, f1, xk, p, d, st);
}
struct FragB { bf16x8 w[2][3]; };
template <int K, int J, int T> __device__ __forceinline__ void rd_b(FragB& f, const lds_cptr (&wpb)[2][3]) {
  constexpr int slot = K >= 8 ? P1_TOP : ((10 + K / 2) & 3) * P1_SLOT;
  constexpr int off = slot + (K & 1) * 12288 + T * 64;
  f.w[J][T] = cat8(lds_tr(wpb[J][off >> 16] + (off & 0xffff)), lds_tr(wpb[J][off >> 16] + (off & 0xffff) + 768));
}
template <int K>
__device__ __forceinline__ void p1_stage_b(f32x16 (&acc)[3], FragB& fn, const FragB& fc, const bf16x8 (&xk)[20], const lds_cptr (&wpb)[2][3],
                                           float (&cb)[6], int par) {
  constexpr int N = K < 9 ? K + 1 : 0;
  mm_a<false>(acc[0], xk[2 * K], fc.w[0][0]);     if (K < 9) { rd_b<N, 0, 0>(fn, wpb); rd_b<N, 0, 1>(fn, wpb); }
  mm_a<false>(acc[1], xk[2 * K], fc.w[0][1]);     if (K < 9) rd_b<N, 0, 2>(fn, wpb);
  mm_a<false>(acc[2], xk[2 * K], fc.w[0][2]);     if (K < 9) rd_b<N, 1, 0>(fn, wpb);
  mm_a<false>(acc[0], xk[2 * K + 1], fc.w[1][0]); if (K < 9) rd_b<N, 1, 1>(fn, wpb);
  mm_a<false>(acc[1], xk[2 * K + 1], fc.w[1][1]); if (K < 9) rd_b<N, 1, 2>(fn, wpb);
  mm_a<false>(acc[2], xk[2 * K + 1], fc.w[1][2]);
  if (par == (K & 1)) { colsum(cb[0], cb[1], fc.w[0][0]); colsum(cb[2], cb[3], fc.w[0][1]); colsum(cb[4], cb[5], fc.w[0][2]); }
}
template <int N> __device__ __forceinline__ void vm_bar() { asm volatile("s_waitcnt vmcnt(%0)\n\ts_barrier" ::"i"(N) : "memory"); }

constexpr int P2_STG = 24576;
constexpr int P3_WO = 81920;
constexpr int P3_C2 = P1_CNT + 16;
template <int T, class HK>
__device__ __forceinline__ void attn_step(HK&& hook, f32x16& Sc, f32x16& Sn, f32x16& o, const bf16x8& q0, const bf16x8& q1, bf16x8& k0, bf16x8& k1,
                                          bf16x8& v0, bf16x8& v1, unsigned (&p)[8], float (&e)[4], lds_cptr kc, lds_cptr kn, lds_cptr vc,
                                          lds_cptr vn, int k1off) {
  const f32x16 zero = {};
  const lds_cptr k2p = (T + 2 < 8) ? kc + (T + 2) * 3072 : kn + (T + 2 - 8) * 3072;
  const lds_cptr v1p = (T + 1 < 8) ? vc + (T + 1) * 3072 : vn;
  float a0, a1, a2, a3;
  Sn = MFMA32(k0, q0, zero);
  k0 = lds_b128(k2p);
  hook(2 * T);
  SB();
  p[6] = cvtpk(e[0], e[1]); p[7] = cvtpk(e[2], e[3]);
  SB();
  a0 = EXP2(Sc[0]); a1 = EXP2(Sc[1]); a2 = EXP2(Sc[2]); a3 = EXP2(Sc[3]);
  SB();
  o = MFMA32(v1, PK4(p[4], p[5], p[6], p[7]), o);
  v1 = lds_b128(vc + T * 3072 + 768);
  SB();
  p[0] = cvtpk(a0, a1); p[1] = cvtpk(a2, a3);
  SB();
  a0 = EXP2(Sc[4]); a1 = EXP2(Sc[5]); a2 = EXP2(Sc[6]); a3 = EXP2(Sc[7]);
  SB();
  Sn = MFMA32(k1, q1, Sn);
  k1 = lds_b128(k2p + k1off);
  hook(2 * T + 1);
  SB();
  p[2] = cvtpk(a0, a1); p[3] = cvtpk(a2, a3);
  SB();
  a0 = EXP2(Sc[8]); a1 = EXP2(Sc[9]); a2 = EXP2(Sc[10]); a3 = EXP2(Sc[11]);
  SB();
  o = MFMA32(v0, PK4(p[0], p[1], p[2], p[3]), o);
  v0 = lds_b128(v1p);
  SB();
  p[4] = cvtpk(a0, a1); p[5] = cvtpk(a2, a3);
  SB();
  e[0] = EXP2(Sc[12]); e[1] = EXP2(Sc[13]); e[2] = EXP2(Sc[14]); e[3] = EXP2(Sc[15]);
  SB();
}

__device__ __forceinline__ int v_colblock(int tile) { return tile < 48 ? tile >> 2 : 12 + (tile - 48) / 3; }

__global__ __launch_bounds__(512, 2) void mega_kernel(const u16* __restrict__ x, const u16* __restrict__ wqkv, const u16* __restrict__ wo,
                                                       const u16* __restrict__ w1, const u16* __restrict__ b1, const u16* __restrict__ w2,
                                                       const u16* __restrict__ b2, char* ws, u16* __restrict__ out) {
  __shared__ __attribute__((aligned(16))) char lds[LDS_BYTES];
  const unsigned lds0 = (unsigned)(uintptr_t)(LDS_AS char*)lds;
  const int b = blockIdx.x, xcd = b & 7, i = b >> 3;
  const int tid = threadIdx.x, lane = tid & 63, w = __builtin_amdgcn_readfirstlane(tid >> 6);
  const int r = lane & 31, hh = lane >> 5, tt = w & 3;
  const auto wsr = __builtin_amdgcn_make_buffer_rsrc(ws, 0, WS_END, 0x00020000);
  STAMP(0);
  const int head = xcd * 4 + (i & 3), qb = i >> 2;
  const int p = (xcd & 1) * 8 + (i & 7), cb = (xcd >> 1) * 4 + (i >> 3);
  const int id1 = p * 16 + cb;
  u64 epoch_v; u32x4 xa, xb;
  {
    const unsigned xo = (unsigned)((p * 128 + cb * 8 + w) * 1280);
    asm volatile("global_load_dwordx2 %0, %3, %6 sc1\n\tglobal_load_dwordx4 %1, %4, %7\n\tglobal_load_dwordx4 %2, %5, %7"
                 : "=&v"(epoch_v), "=&v"(xa), "=&v"(xb) : "v"(WS_SYNC), "v"(xo + lane * 16), "v"(xo + 1024 + (lane & 15) * 16), "s"(ws), "s"(x) : "memory");
  }
  {
    u64 pc; asm volatile("s_getpc_b64 %0" : "=s"(pc));
    if (w == 6) glds4((unsigned)min((i * 14 + min(lane, 13)) * 128, PFC), (const char*)(pc & ~255ull), lds0 + P1_TCH);
  }
  if (tid < 8) ((LDS_AS unsigned*)(LDS_AS char*)&lds[P1_CNT])[tid] = 0u;
  u64 epoch;
  const char* kvg = ws + WS_KV + head * 196608;
  const unsigned vdma = (unsigned)(w * 3072 + lane * 16);
  const unsigned qo = WS_Q + (head * 64 + qb * 8 + w) * 1536;
  bf16x8 q0; u32x4 q1u;

  {
    const int qt0 = cb < 12 ? cb * 4 : 48 + (cb - 12) * 3;
    const int nq = cb < 12 ? 4 : 3;
    const int wh = w >> 2, nt = w >> 1, tp = w & 1;
    const int tk = 2 * tp + (nt & 1), to = tk ^ 1;
    P1Dma d;
    if (w >= 4) {
      const int qr = 4 * tt + (lane >> 4);
#pragma unroll
      for (int q = 0; q < 4; ++q)
        d.vo[q] = (unsigned)((qr + 16 * q) * 3840 + (qt0 + min((((lane >> 2) & 3) ^ (qr & 3)), nq - 1)) * 64 + (lane & 3) * 16);
      d.base = (const char*)wqkv; d.kstride = 64 * 3840; d.ldsw = lds0 + P1_W + tt * 1024; d.lstep = 4096;
    } else {
#pragma unroll
      for (int q = 0; q < 4; ++q) {
        const int row = 32 * tt + 8 * q + (lane >> 3);
        d.vo[q] = (unsigned)((p * 128 + row) * 1280 + (((lane & 7) ^ ((row >> 1) & 7)) << 4));
      }
      d.base = (const char*)x; d.kstride = 128; d.ldsw = lds0 + tt * 4096; d.lstep = 1024;
    }
    if (w == 7) glds4((unsigned)(cb * 192 + min(lane, 47) * 4), b1, lds0 + P1_B1);
    p1_issue<0>(d);
    d.wm = (const char*)w1 + wh * (64 * 3072); d.ldsm = lds0 + wh * 12288 + tt * 1024;
#pragma unroll
    for (int q = 0; q < 3; ++q) { const int u = q * 256 + tt * 64 + lane; d.vm[q] = (unsigned)((u / 12) * 3072 + cb * 192 + (u % 12) * 16); }
    asm volatile("s_waitcnt vmcnt(" PFW ")" : "+v"(epoch_v), "+v"(xa), "+v"(xb)::"memory");
    epoch = rfl64(epoch_v) + 1;
    const unsigned tag = (unsigned)epoch ^ (unsigned)(epoch >> 32);
    {
      const float c = __uint_as_float(__builtin_amdgcn_readfirstlane(xa[0]) << 16);
      float s1 = 0.f, s2 = 0.f, t1 = 0.f, t2 = 0.f;
#pragma unroll
      for (int e = 0; e < 4; ++e) {
        const float a0 = bflo(xa[e]) - c, a1 = bfhi(xa[e]) - c, b0 = bflo(xb[e]) - c, b1v = bfhi(xb[e]) - c;
        s1 += a0 + a1; s2 = fmaf(a0, a0, fmaf(a1, a1, s2)); t1 += b0 + b1v; t2 = fmaf(b0, b0, fmaf(b1v, b1v, t2));
      }
      if (lane < 16) { s1 += t1; s2 += t2; }
      const float m = wave_sum(s1) * (1.0f / 640.0f);
      const float var = fmaxf(wave_sum(s2) * (1.0f / 640.0f) - m * m, 0.0f);
      const float r1 = __builtin_amdgcn_rsqf(var + EPS);
      const float r2 = __builtin_amdgcn_rsqf(var * r1 * r1 + EPS);
      ST16(((u32x4){__float_as_uint(m + c), __float_as_uint(r1), __float_as_uint(r1 * r2), tag}), lane == 0 ? WS_RS + (p * 128 + cb * 8 + w) * 16 : OOB);
    }
    STAMP(1);
    p1_issue<1>(d); p1_issue<2>(d); p1_issue<3>(d);
    PtrA pa;
    {
      if (w < 3) {
        const int v = w * 64 + lane;
        pa.tv = (unsigned)((p * 128 + (i >> 3) * 32 + min(v / 5, 31)) * 1280 + 640 + (v % 5) * 128); pa.tb = (const char*)x;
      } else if (w < 5) {
        const int v = (w - 3) * 64 + lane;
        pa.tv = (unsigned)((320 + (i & 7) * 40 + min(v / 3, 39)) * 3840 + min(qt0 * 64 + (v % 3) * 124, 3836)); pa.tb = (const char*)wqkv;
      } else {
        const int v = (w - 5) * 64 + lane;
        pa.tv = (unsigned)(((i & 7) * 80 + min(v >> 1, 79)) * 3072 + cb * 192 + (v & 1) * 188); pa.tb = (const char*)w1;
      }
      pa.tl = lds0 + P1_TCH;
    }
    {
      const int q4 = (lane & 15) >> 2;
      const lds_cptr wb = (lds_cptr)lds + P1_W + (8 * hh + q4) * 256 + (nt ^ q4) * 64 + ((lane >> 4) & 1) * 32 + (lane & 3) * 8;
#pragma unroll
      for (int j = 0; j < 4; ++j) {
        const int ks = 2 * ((j >> 1) ^ wh) + ((j & 1) ^ tp);
        const int cs = ((2 * ks + hh) ^ ((r >> 1) & 7)) << 4;
        pa.x0[j] = (lds_cptr)lds + (tk * 32 + r) * 128 + cs;
        pa.x1[j] = (lds_cptr)lds + (to * 32 + r) * 128 + cs;
        pa.w[j] = wb + ks * 4096;
      }
      pa.csw = (LDS_AS float*)(LDS_AS char*)&lds[P1_CSA] + w * 64 + lane;
      pa.ws = ws; pa.g0 = WS_RS + (p * 128 + tk * 32 + r) * 16; pa.g1 = WS_RS + (p * 128 + to * 32 + r) * 16;
    }
    lds_cptr wpb[2][3];
#pragma unroll
    for (int j = 0; j < 2; ++j) {
      wpb[j][0] = (lds_cptr)lds + (32 * wh + 16 * (j ^ tp) + 8 * hh + ((lane & 15) >> 2)) * 192 + (((lane >> 4) & 1) ^ wh) * 32 + (lane & 3) * 8;
      wpb[j][1] = wpb[j][0] + 65536; wpb[j][2] = wpb[j][0] + 131072;
    }
    bf16x8 xk[20];
    FragB g0, g1;
    StatA st;
    st.lo = 0.f; st.hi = 0.f;
    {
      f32x16 acc[2] = {};
      if (cb >= 10) p1_pass_a<true>(acc, xk, pa, d, st);
      else p1_pass_a<false>(acc, xk, pa, d, st);
      STAMP(3);
      const float csp = ((LDS_AS float*)(LDS_AS char*)&lds[P1_CSA])[(w ^ 1) * 64 + lane];
      rd_b<0, 0, 0>(g0, wpb); rd_b<0, 0, 1>(g0, wpb); rd_b<0, 0, 2>(g0, wpb); rd_b<0, 1, 0>(g0, wpb); rd_b<0, 1, 1>(g0, wpb); rd_b<0, 1, 2>(g0, wpb);
      SB();
      if (__builtin_expect(!__all(st.gk[3] == tag && st.go[3] == tag), 0)) {
#pragma nounroll
        for (unsigned sp = 0; sp < SPIN_CAP; ++sp) {
          st.gk = __builtin_bit_cast(u32x4, LD16(pa.g0, 0)); st.go = __builtin_bit_cast(u32x4, LD16(pa.g1, 0));
          if (__all(st.gk[3] == tag && st.go[3] == tag)) break;
          __builtin_amdgcn_s_sleep(3);
          asm volatile("" ::: "memory");
        }
      }
      const bf16x8 csf = split_a(tp ? csp + st.lo : st.lo + csp);
      const bf16x8 mk = split_b(-__uint_as_float(st.gk[0])), mo = split_b(-__uint_as_float(st.go[0]));
      if (cb >= 10) {
        acc[0] = MFMA32(mk, csf, acc[0]); acc[1] = MFMA32(mo, csf, acc[1]);
        LDS_AS float* rsv = (LDS_AS float*)(LDS_AS char*)&lds[P1_RSV] + w * 64;
        rsv[lane] = __uint_as_float(hh ? st.go[1] : st.gk[1]);
        const int n = (qt0 + nt) * 32 + r - 1280;
        const int hd = n / 20, dd = n - hd * 20;
        const unsigned ov = dd == 18 ? 0x3f803f80u : 0u;
#pragma unroll
        for (int t = 0; t < 2; ++t) {
          unsigned pk[8];
#pragma unroll
          for (int g = 0; g < 4; ++g) {
            const f32x4 rr = *(const LDS_AS f32x4*)(rsv + t * 32 + 8 * g + 4 * hh);
            pk[2 * g] = cvtpk(acc[t][4 * g] * rr[0], acc[t][4 * g + 1] * rr[1]); pk[2 * g + 1] = cvtpk(acc[t][4 * g + 2] * rr[2], acc[t][4 * g + 3] * rr[3]);
          }
          const unsigned vp = nt < nq ? WS_KV + hd * 196608 + (p * 4 + (t ? to : tk)) * 3072 + 1536 + (hh * 24 + dd) * 16 : OOB;
          ST16(((u32x4){pk[0], pk[1], pk[2], pk[3]}), vp);
          ST16(((u32x4){pk[4], pk[5], pk[6], pk[7]}), vp + 768);
          const unsigned vq = dd >= 18 ? vp + 32 : OOB;
          ST16(((u32x4){ov, ov, ov, ov}), vq);
          ST16(((u32x4){ov, ov, ov, ov}), vq + 768);
        }
      } else {
        acc[0] = MFMA32(csf, mk, acc[0]); acc[1] = MFMA32(csf, mo, acc[1]);
        const bool isq = cb < 5;
        const float qs = isq ? QSCALE : 1.0f;
        const float rs2[2] = {qs * __uint_as_float(st.gk[1]), qs * __uint_as_float(st.go[1])};
        const unsigned ts = isq ? 1536u : 3072u;
        const unsigned base = (isq ? WS_Q : WS_KV) + p * 4 * ts + r * 16;
        const unsigned hs = isq ? 64u * 1536u : 196608u;
        const unsigned cst = isq ? 0u : 0x3f80u;
#pragma unroll
        for (int g = 0; g < 4; g += 2) {
          const unsigned os = (qt0 + nt) * 4 + g + hh - (isq ? 0 : 80);
          const unsigned hp = os / 5u, oi = os - hp * 5u;
          const bool sp = oi == 2;
          const unsigned a0 = base + (2 * hp + (oi > 2)) * hs + (oi > 2 ? oi - 3 : oi) * 512;
#pragma unroll
          for (int t = 0; t < 2; ++t) {
            const float rs = rs2[t];
            const auto s0 = __builtin_amdgcn_permlane32_swap(cvtpk(acc[t][4 * g] * rs, acc[t][4 * g + 1] * rs),
                                                             cvtpk(acc[t][4 * g + 4] * rs, acc[t][4 * g + 5] * rs), false, false);
            const auto s1 = __builtin_amdgcn_permlane32_swap(cvtpk(acc[t][4 * g + 2] * rs, acc[t][4 * g + 3] * rs),
                                                             cvtpk(acc[t][4 * g + 6] * rs, acc[t][4 * g + 7] * rs), false, false);
            const unsigned a1 = a0 + (t ? to : tk) * ts;
            ST16(((u32x4){s0[0], s1[0], sp ? cst : s0[1], sp ? 0u : s1[1]}), a1);
            ST16(((u32x4){s0[1], s1[1], cst, 0u}), sp ? a1 + hs : OOB);
          }
        }
      }
      SB();
    }
    STAMP(16);
    const float mean_k = __uint_as_float(st.gk[0]), rs = __uint_as_float(st.gk[2]);
    f32x16 acc[3] = {};
    float cbs[6] = {};
    const int par = (nt & 1) ^ wh;
    p1_stage_b<0>(acc, g1, g0, xk, wpb, cbs, par);
    vm_bar<14>();
    const int bk = w * 256 + b;
    const int rto = w == 0 ? 24576 : w == 1 ? 57344 : 65536;
    if (bk < 544) {
      const int kk = bk % 68, c8 = bk / 68;
      const int r0 = kk < 48 ? 32 * kk : kk < 64 ? 40 * (kk - 48) : 160 * (kk - 64) + 16;
      const int ra = kk < 48 ? 16 : kk < 64 ? 20 : 80, rbb = kk < 64 ? 8 : 20, rc = kk < 64 ? 4 : 40;
#pragma unroll
      for (int q = 0; q < 5; ++q) {
        const int u = q * 64 + lane, rho = u / 10;
        const int row = r0 + ra * (rho >> 4) + rc * ((rho >> 3) & 1) + rbb * ((rho >> 2) & 1) + (rho & 3);
        glds16((unsigned)(row * 1280 + c8 * 160 + (u % 10) * 16), kk < 48 ? w2 : wo, lds0 + rto + q * 1024);
      }
    }
    p1_stage_b<1>(acc, g0, g1, xk, wpb, cbs, par); p1_stage_b<2>(acc, g1, g0, xk, wpb, cbs, par);
    if (bk < 544) vm_bar<16>(); else vm_bar<11>();
    STAMP(17);
    p1_stage_b<3>(acc, g0, g1, xk, wpb, cbs, par); p1_stage_b<4>(acc, g1, g0, xk, wpb, cbs, par);
    STAMP(18);
    if (bk < 544) vm_bar<5>(); else vm_bar<0>();
    STAMP(19);
    if (tid == 0) st_flag(ws, WS_F1 + id1 * 128, epoch);
    p1_stage_b<5>(acc, g0, g1, xk, wpb, cbs, par); p1_stage_b<6>(acc, g1, g0, xk, wpb, cbs, par);
    p1_stage_b<7>(acc, g0, g1, xk, wpb, cbs, par);
    if (bk < 544) {
      wait_vm<0>();
      const lds_cptr rp = (lds_cptr)lds + rto + (8 * (lane >> 4) + ((lane & 15) >> 2)) * 160 + (lane & 3) * 8;
#pragma unroll
      for (int t = 0; t < 5; ++t)
        ST16(__builtin_bit_cast(u32x4, cat8(lds_tr(rp + t * 32), lds_tr(rp + t * 32 + 640))), WS_WT + (bk * 5 + t) * 1024 + lane * 16);
    }
    unsigned fo1 = 0, fo2 = 0;
    u64 fa = 0, fb = 0;
    if (w == 3) {
      const int ca = (head * 20) >> 7, cbb = (head * 20 + 19) >> 7;
      const int va = v_colblock(40 + ((head * 20) >> 5)), vb = v_colblock(40 + ((head * 20 + 19) >> 5));
      const int sel = lane >> 4;
      const int c1 = sel == 0 ? 5 + ca : sel == 1 ? 5 + cbb : sel == 2 ? va : vb;
      fo1 = WS_F1 + ((lane & 15) * 16 + c1) * 128;
      fo2 = WS_F1 + ((2 * qb + (lane & 1)) * 16 + ((lane & 2) ? cbb : ca)) * 128;
      fa = ld_flag(ws, fo1); fb = ld_flag(ws, fo2);
    }
    p1_stage_b<8>(acc, g1, g0, xk, wpb, cbs, par);
    p1_stage_b<9>(acc, g0, g1, xk, wpb, cbs, par);
    STAMP(4);
    LDS_AS f32x4* xch = (LDS_AS f32x4*)(LDS_AS char*)&lds[73728];
    LDS_AS float* cst = (LDS_AS float*)(LDS_AS char*)&lds[73728 + 49152];
    const int rsw = lane ^ (wh << 4);
#pragma unroll
    for (int t = 0; t < 3; ++t) {
      cst[(t * 64 + rsw) * 8 + w] = cbs[2 * t] + cbs[2 * t + 1];
#pragma unroll
      for (int u = 0; u < 2; ++u)
        xch[(w * 6 + 2 * t + u) * 64 + lane] = (f32x4){acc[t][8 + 4 * u], acc[t][9 + 4 * u], acc[t][10 + 4 * u], acc[t][11 + 4 * u]};
    }
    if (w == 3) { SB(); if (!__all(fa == epoch && fb == epoch)) poll2(ws, fo1, fo2, epoch); SB(); }
    bar_lds();
    STAMP(20);
    f32x4 c0[3], c1[3], px[6];
    u32x2 bb[6];
#pragma unroll
    for (int t = 0; t < 3; ++t) { c0[t] = *(const LDS_AS f32x4*)(cst + (t * 64 + rsw) * 8); c1[t] = *(const LDS_AS f32x4*)(cst + (t * 64 + rsw) * 8 + 4); }
#pragma unroll
    for (int q = 0; q < 6; ++q) {
      px[q] = xch[((w ^ 4) * 6 + q) * 64 + lane];
      bb[q] = *(const u32x2*)(lds + P1_B1 + ((q >> 1) * 32 + 16 * wh + 8 * (q & 1) + 4 * hh) * 2);
    }
    SB();
    glds16x3(vdma, kvg, lds0 + w * 3072);
    q0 = LD16(qo + lane * 16, 0);
    q1u = __builtin_bit_cast(u32x4, LD16(qo + 1024 + r * 16, 0));
    SB();
    glds16x3(vdma, kvg + P2_STG, lds0 + P2_STG + w * 3072);
    glds16x3(vdma, kvg + 2 * P2_STG, lds0 + 2 * P2_STG + w * 3072);
    SB();
    const unsigned hp = WS_H + ((p * 4 + tk) * 96 + cb * 6 + wh) * 1024 + lane * 16;
    const bf16x8 mk = split_b(-mean_k);
#pragma unroll
    for (int t = 0; t < 3; ++t) {
      const f32x4 cc = c0[t] + c1[t];
      acc[t] = MFMA32(split_a((cc[0] + cc[1]) + (cc[2] + cc[3])), mk, acc[t]);
    }
#pragma unroll
    for (int t = 0; t < 3; ++t) {
      unsigned pk[4];
#pragma unroll
      for (int u = 0; u < 2; ++u) {
        const f32x4 a = (f32x4){acc[t][4 * u], acc[t][4 * u + 1], acc[t][4 * u + 2], acc[t][4 * u + 3]} + px[2 * t + u];
        const u32x2 bq = bb[2 * t + u];
        pk[2 * u] = gelu2((f32x2){a[0], a[1]} * rs + (f32x2){bflo(bq[0]), bfhi(bq[0])});
        pk[2 * u + 1] = gelu2((f32x2){a[2], a[3]} * rs + (f32x2){bflo(bq[1]), bfhi(bq[1])});
      }
      ST16(((u32x4){pk[0], pk[1], pk[2], pk[3]}), hp + t * 2048);
    }
    STAMP(21);
  }

  const int rb = xcd * 4 + (i & 3), cq = i >> 2;
  const unsigned ao = (lane >> 5) * 1024 + (((lane >> 4) & 1) * 32 + (lane & 15)) * 16;
  const unsigned wofs = WS_WT + cq * 68 * 5 * 1024 + lane * 16;
  bf16x8 af[3][4], wf[3][5];
  auto ldha = [&](int s, int kk) {
#pragma unroll
    for (int m = 0; m < 4; ++m) af[s][m] = LD16(ao + (m & 1) * 256, WS_H + ((2 * rb + (m >> 1)) * 96 + 2 * kk) * 1024);
  };
  auto ldw = [&](int s, int kk) {
#pragma unroll
    for (int t = 0; t < 5; ++t) wf[s][t] = LD16(wofs + t * 1024, kk * 5120);
  };
  auto ldh = [&](int s, int kk) { ldha(s, kk); ldw(s, kk); };
  const int kh0 = w < 4 ? 9 * w : 36 + 3 * (w - 4);
  auto wo_piece = [&](int q) {
    const int n = min(w * 8 + q, 59), j = n / 15;
    glds16(wofs, ws + (48 + 5 * j) * 5120 + (n - 15 * j) * 1024, lds0 + P3_WO + n * 1024);
  };
  {
    if (hh) q1u = (u32x4){0u, 0u, 0u, 0u};
    const int dcl = r < 21 ? r : 21;
    const int k1off = hh ? 1536 + 21 * 16 - lane * 16 : 1024;
    const lds_cptr kb = (lds_cptr)lds + lane * 16;
    const lds_cptr vb = (lds_cptr)lds + 1536 + (hh * 24 + dcl) * 16;
    const f32x16 zero = {};
    wait_vm_bar<6>();
    STAMP(22);
    bf16x8 k0 = lds_b128(kb), k1 = lds_b128(kb + k1off);
    f32x16 sa = MFMA32(k0, q0, zero), sb;
    {
      f32x16 s = MFMA32(k1, __builtin_bit_cast(bf16x8, q1u), sa);
      float m = s[0];
#pragma unroll
      for (int q = 1; q < 16; ++q) m = fmaxf(m, s[q]);
      m = xhalf_max(m);
      q1u[2] = hh ? 0u : (cvtpk(-m, 0.0f) & 0xffffu);
    }
    const bf16x8 q1 = __builtin_bit_cast(bf16x8, q1u);
    sa = MFMA32(k1, q1, sa);
    k0 = lds_b128(kb + 3072); k1 = lds_b128(kb + 3072 + k1off);
    bf16x8 v0 = lds_b128(vb), v1 = {};
    unsigned pp[8] = {};
    float e[4] = {};
    f32x16 o = {};
    int sc = 0;
    u64 fl = 0;
    const unsigned flo = WS_F1B + (w * 32 + r) * 128;
    __builtin_amdgcn_s_setprio(2);
    STAMP(6);
#define ATTN_STAGE(HOOK) do { \
      const int sn = sc == 2 * P2_STG ? 0 : sc + P2_STG; \
      const lds_cptr kc = kb + sc, kn = kb + sn, vc = vb + sc, vn = vb + sn; \
      attn_step<0>(HOOK, sa, sb, o, q0, q1, k0, k1, v0, v1, pp, e, kc, kn, vc, vn, k1off); \
      attn_step<1>(HOOK, sb, sa, o, q0, q1, k0, k1, v0, v1, pp, e, kc, kn, vc, vn, k1off); \
      attn_step<2>(HOOK, sa, sb, o, q0, q1, k0, k1, v0, v1, pp, e, kc, kn, vc, vn, k1off); \
      attn_step<3>(HOOK, sb, sa, o, q0, q1, k0, k1, v0, v1, pp, e, kc, kn, vc, vn, k1off); \
      attn_step<4>(HOOK, sa, sb, o, q0, q1, k0, k1, v0, v1, pp, e, kc, kn, vc, vn, k1off); \
      attn_step<5>(HOOK, sb, sa, o, q0, q1, k0, k1, v0, v1, pp, e, kc, kn, vc, vn, k1off); \
      attn_step<6>(HOOK, sa, sb, o, q0, q1, k0, k1, v0, v1, pp, e, kc, kn, vc, vn, k1off); \
      attn_step<7>(HOOK, sb, sa, o, q0, q1, k0, k1, v0, v1, pp, e, kc, kn, vc, vn, k1off); \
      scp = sc; sc = sn; } while (0)
    int scp;
#pragma nounroll
    for (int st = 0; st < 6; ++st) {
      ATTN_STAGE([](int) {});
      if (st == 5 && !__all(fl == epoch)) poll2(ws, flo, flo, epoch);
      asm volatile("s_waitcnt vmcnt(0) lgkmcnt(2)\n\ts_barrier" ::: "memory");
      if (st < 5) glds16x3(vdma, kvg + (st + 3) * P2_STG, lds0 + scp + w * 3072);
      if (st == 4) fl = ld_flag(ws, flo);
      if (st == 0 && tid == 0) st_flag(ws, WS_F1B + b * 128, epoch);
      SB();
    }
    auto pf = [&](int n) {
      if (n < 27) {
        const int s = n / 9, j = n % 9, kk = kh0 + s;
        if (j < 4) af[s][j] = LD16(ao + (j & 1) * 256, WS_H + ((2 * rb + (j >> 1)) * 96 + 2 * kk) * 1024);
        else wf[s][j - 4] = LD16(wofs + (j - 4) * 1024, kk * 5120);
      } else {
        wo_piece(n - 24);
      }
    };
    wo_piece(0); wo_piece(1); wo_piece(2);
    ATTN_STAGE([&](int n) { pf(n); });
    ATTN_STAGE([&](int n) { pf(n + 16); });
    STAMP(7);
    pp[6] = cvtpk(e[0], e[1]); pp[7] = cvtpk(e[2], e[3]);
    o = MFMA32(v1, PK4(pp[4], pp[5], pp[6], pp[7]), o);
    float l = upper_half(o[8]);
    float amax = 0.f;
#pragma unroll
    for (int q = 0; q < 12; ++q) amax = fmaxf(amax, fabsf(o[q]));
    const bool bad = !(l < 1.0e9f) || !(l > 0.25f) || !(amax < 1.0e30f);
    if (__builtin_expect(__any(bad), 0)) {
      q1u[2] = 0u;
      const bf16x8 q1z = __builtin_bit_cast(bf16x8, q1u);
      float m = -INFINITY;
      o = zero;
#pragma nounroll
      for (int t = 0; t < 64; ++t) {
        const unsigned tp = WS_KV + head * 196608 + t * 3072;
        f32x16 s = MFMA32(LD16(tp + lane * 16, 0), q0, zero);
        s = MFMA32(LD16(tp + k1off + lane * 16, 0), q1z, s);
        float tm = s[0];
#pragma unroll
        for (int q = 1; q < 16; ++q) tm = fmaxf(tm, s[q]);
        tm = xhalf_max(tm);
        const float mn = fmaxf(m, tm);
        const float al = EXP2(m - mn);
        m = mn;
        unsigned pk[8];
#pragma unroll
        for (int q = 0; q < 8; ++q) pk[q] = cvtpk(EXP2(s[2 * q] - mn), EXP2(s[2 * q + 1] - mn));
#pragma unroll
        for (int q = 0; q < 16; ++q) o[q] *= al;
        const unsigned vp = tp + 1536 + (hh * 24 + dcl) * 16;
        o = MFMA32(LD16(vp, 0), PK4(pk[0], pk[1], pk[2], pk[3]), o);
        o = MFMA32(LD16(vp + 768, 0), PK4(pk[4], pk[5], pk[6], pk[7]), o);
      }
      l = upper_half(o[8]);
    }
    const float il = 1.0f / l;
    const unsigned op = WS_O + (qb * 8 + w) * 40 * 1024;
    ST16(((u32x4){cvtpk(o[0] * il, o[1] * il), cvtpk(o[2] * il, o[3] * il), cvtpk(o[4] * il, o[5] * il), cvtpk(o[6] * il, o[7] * il)}),
         op + head * 1024 + lane * 16);
    ST8(((u32x2){cvtpk(o[8] * il, o[9] * il), cvtpk(o[10] * il, o[11] * il)}),
        hh ? OOB : op + (32 + (head >> 2)) * 1024 + (((head >> 1) & 1) * 32 + r) * 16 + (head & 1) * 8);
    STAMP(8);
  }

  {
    const int g = lane >> 4, c = lane & 15;
    f32x4 acc[4][5] = {};
    LDS_AS f32x4* red = (LDS_AS f32x4*)(LDS_AS char*)lds;
    LDS_AS unsigned* cnt = (LDS_AS unsigned*)(LDS_AS char*)&lds[P1_CNT];
    auto mm = [&](int s) {
      SB();
#pragma unroll
      for (int m = 0; m < 4; ++m)
#pragma unroll
        for (int t = 0; t < 5; ++t) acc[m][t] = MFMA16(wf[s][t], af[s][m], acc[m][t]);
      SB();
    };
    wait_vm<0>();
    {
      unsigned old = 0;
      if (lane == 0) old = __hip_atomic_fetch_add(cnt, 1u, __ATOMIC_RELAXED, __HIP_MEMORY_SCOPE_WORKGROUP);
      if (lane == 0 && old == 7) {
        st_flag(ws, WS_F2 + (qb * 32 + head) * 128, epoch);
        if (b == 0) st_flag(ws, WS_SYNC, epoch);
      }
    }
    STAMP(13);
    __builtin_amdgcn_s_setprio(0);
    mm(0);
    u32x2 xv[3] = {}, bv[3] = {};
    auto ldres = [&]() {
#pragma unroll
      for (int u = 0; u < 3; ++u) {
        const int tl = min(w + 8 * u, 19), m = tl / 5, t = tl - m * 5;
        xv[u] = *(const u32x2*)(x + (size_t)(rb * 64 + m * 16 + c) * 640 + cq * 80 + t * 16 + 4 * g);
        bv[u] = *(const u32x2*)(b2 + cq * 80 + t * 16 + 4 * g);
      }
    };
    const int j = w & 3;
    ldw(0, w < 4 ? kh0 + 3 : 48 + 5 * j + 3);
    if (w < 4) ldha(0, kh0 + 3);
    mm(1);
    ldw(1, w < 4 ? kh0 + 4 : 48 + 5 * j + 4);
    if (w < 4) ldha(1, kh0 + 4);
    mm(2);
    if (w < 4) {
      ldh(2, kh0 + 5);
      mm(0); ldh(0, kh0 + 6);
      mm(1); ldh(1, kh0 + 7);
      mm(2); ldh(2, kh0 + 8);
      mm(0); ldres();
      mm(1); mm(2);
      STAMP(14);
      lds_wait(lds0 + P1_CNT, 8u);
#pragma unroll
      for (int m = 0; m < 4; ++m)
#pragma unroll
        for (int t = 0; t < 5; ++t) red[(w * 20 + m * 5 + t) * 64 + lane] = acc[m][t];
      asm volatile("s_waitcnt lgkmcnt(0)" ::: "memory");
      __hip_atomic_store(cnt + 4 + w, 1u, __ATOMIC_RELAXED, __HIP_MEMORY_SCOPE_WORKGROUP);
      STAMP(10);
    }
    int w2 = w;
    asm volatile("" : "+s"(w2));
    if (w2 >= 4) {
      STAMP(14);
      STAMP(15);
      {
        const unsigned fo = WS_F2 + ((rb >> 2) * 32 + (lane & 31)) * 128;
        poll2(ws, fo, fo, epoch);
      }
      STAMP(9);
      bf16x8 of[5][4];
#pragma unroll
      for (int k = 0; k < 5; ++k)
#pragma unroll
        for (int m = 0; m < 4; ++m) of[k][m] = LD16(ao + (m & 1) * 256, WS_O + ((2 * rb + (m >> 1)) * 40 + 2 * (5 * j + (k + 3) % 5)) * 1024);
      SB();
      lds_wait(lds0 + P1_CNT, 8u);
      const lds_cptr wol = (lds_cptr)lds + P3_WO + j * 15360 + lane * 16;
#pragma unroll
      for (int k = 0; k < 5; ++k) {
        if (k >= 1 && k < 4) {
#pragma unroll
          for (int t = 0; t < 5; ++t) wf[(k + 1) & 1][t] = lds_b128(wol + ((k - 1) * 5 + t) * 1024);
        }
        if (k == 2) ldres();
        SB();
#pragma unroll
        for (int m = 0; m < 4; ++m)
#pragma unroll
          for (int t = 0; t < 5; ++t) acc[m][t] = MFMA16(wf[k & 1][t], of[k][m], acc[m][t]);
        SB();
      }
      STAMP(10);
      lds_wait(lds0 + P1_CNT + 16 + 4 * j, 1u);
#pragma unroll
      for (int m = 0; m < 4; ++m) {
#pragma unroll
        for (int t = 0; t < 5; ++t) red[(j * 20 + m * 5 + t) * 64 + lane] += acc[m][t];
      }
    }
    bar_lds();
    STAMP(11);
#pragma unroll
    for (int u = 0; u < 3; ++u) {
      const int tl = w + 8 * u;
      if (tl < 20) {
        const int m = tl / 5, t = tl - m * 5;
        f32x4 a = red[tl * 64 + lane];
#pragma unroll
        for (int q = 1; q < 4; ++q) a += red[(q * 20 + tl) * 64 + lane];
        *(u32x2*)(out + (size_t)(rb * 64 + m * 16 + c) * 640 + cq * 80 + t * 16 + 4 * g) =
            (u32x2){cvtpk(a[0] + bflo(xv[u][0]) + bflo(bv[u][0]), a[1] + bfhi(xv[u][0]) + bfhi(bv[u][0])),
                    cvtpk(a[2] + bflo(xv[u][1]) + bflo(bv[u][1]), a[3] + bfhi(xv[u][1]) + bfhi(bv[u][1]))};
      }
    }
    STAMP(12);
  }
}

extern "C" void kernel_launch(void* const* d_in, const int* in_sizes, int n_in, void* d_out, int out_size, void* d_ws, size_t ws_size,
                              hipStream_t stream) {
  mega_kernel<<<256, 512, 0, stream>>>((const u16*)d_in[0], (const u16*)d_in[1], (const u16*)d_in[2], (const u16*)d_in[4], (const u16*)d_in[5],
                                       (const u16*)d_in[6], (const u16*)d_in[7], (char*)d_ws, (u16*)d_out);
}
```

```cpp
#define PFC 53888
#include <hip/hip_runtime.h>
#include <hip/hip_bf16.h>
#include <stdint.h>

typedef unsigned short u16;
typedef unsigned long long u64;
typedef short bf16x8 __attribute__((ext_vector_type(8)));
typedef short s16x4 __attribute__((ext_vector_type(4)));
typedef float f32x16 __attribute__((ext_vector_type(16)));
typedef float f32x4 __attribute__((ext_vector_type(4)));
typedef float f32x2 __attribute__((ext_vector_type(2)));
typedef unsigned u32x4 __attribute__((ext_vector_type(4)));
typedef unsigned u32x2 __attribute__((ext_vector_type(2)));
typedef __bf16 bf2 __attribute__((ext_vector_type(2)));
#define LDS_AS __attribute__((address_space(3)))
#define GAS __attribute__((address_space(1)))
typedef LDS_AS const char* lds_cptr;
typedef GAS u64 gu64;
#define RLX_AGENT __ATOMIC_RELAXED, __HIP_MEMORY_SCOPE_AGENT

#define PFW "4"
constexpr float EPS = 1e-5f;
constexpr float QSCALE = 0.22360679774997896f * 1.4426950408889634f;

constexpr unsigned WS_SYNC = 0;
constexpr unsigned WS_F0 = 4096, WS_F1 = WS_F0 + 32768, WS_F1B = WS_F1 + 32768, WS_F2 = WS_F1B + 32768;
constexpr unsigned WS_Q = WS_F2 + 32768;
constexpr unsigned WS_KV = WS_Q + 32 * 64 * 1536;
constexpr unsigned WS_O = WS_KV + 32 * 196608;
constexpr unsigned WS_H = WS_O + 64 * 40 * 1024;
constexpr unsigned WS_XN = WS_H + 64 * 96 * 1024;
constexpr unsigned WS_RS = WS_XN;
constexpr unsigned WS_WT = WS_RS + 32768;
constexpr unsigned WS_END = WS_WT + 8 * 68 * 5 * 1024;

#define MFMA32(a, b, c) __builtin_amdgcn_mfma_f32_32x32x16_bf16(a, b, c, 0, 0, 0)
#define MFMA16(a, b, c) __builtin_amdgcn_mfma_f32_16x16x32_bf16(a, b, c, 0, 0, 0)
#define SB() __builtin_amdgcn_sched_barrier(0)
#define EXP2(v) __builtin_amdgcn_exp2f(v)
#define PK4(a, b, c, d) __builtin_bit_cast(bf16x8, ((u32x4){a, b, c, d}))
#define ST16(v, off) __builtin_amdgcn_raw_buffer_store_b128(v, wsr, off, 0, 16)
#define ST8(v, off) __builtin_amdgcn_raw_buffer_store_b64(v, wsr, off, 0, 16)
#define LD16(off, soff) __builtin_bit_cast(bf16x8, __builtin_amdgcn_raw_buffer_load_b128(wsr, off, soff, 16))

#define STAMP(i)

__device__ __forceinline__ unsigned cvtpk(float lo, float hi) {
  return __builtin_bit_cast(unsigned, __builtin_convertvector((f32x2){lo, hi}, bf2));
}
__device__ __forceinline__ float bflo(unsigned u) { return __uint_as_float(u << 16); }
__device__ __forceinline__ float bfhi(unsigned u) { return __uint_as_float(u & 0xffff0000u); }
__device__ __forceinline__ void glds4(unsigned voff, const void* sbase, unsigned ldsb) {
  unsigned sv;
  asm volatile("s_mov_b32 %0, m0\n\ts_mov_b32 m0, %3\n\ts_nop 0\n\tglobal_load_lds_dword %1, %2 sc1\n\ts_mov_b32 m0, %0"
               : "=&s"(sv) : "v"(voff), "s"(sbase), "s"(ldsb) : "memory");
}
__device__ __forceinline__ void glds16x3(unsigned voff, const void* sbase, unsigned ldsb) {
  unsigned sv;
  asm volatile("s_mov_b32 %0, m0\n\ts_mov_b32 m0, %3\n\ts_nop 0\n\t"
               "global_load_lds_dwordx4 %1, %2 sc1\n\t"
               "global_load_lds_dwordx4 %1, %2 offset:1024 sc1\n\t"
               "global_load_lds_dwordx4 %1, %2 offset:2048 sc1\n\t"
               "s_mov_b32 m0, %0"
               : "=&s"(sv) : "v"(voff), "s"(sbase), "s"(ldsb) : "memory");
}
__device__ __forceinline__ void glds16x4(unsigned voff, const void* sbase, unsigned ldsb) {
  unsigned sv;
  asm volatile("s_mov_b32 %0, m0\n\ts_mov_b32 m0, %3\n\ts_nop 0\n\t"
               "global_load_lds_dwordx4 %1, %2 sc1\n\t"
               "global_load_lds_dwordx4 %1, %2 offset:1024 sc1\n\t"
               "global_load_lds_dwordx4 %1, %2 offset:2048 sc1\n\t"
               "global_load_lds_dwordx4 %1, %2 offset:3072 sc1\n\t"
               "s_mov_b32 m0, %0"
               : "=&s"(sv) : "v"(voff), "s"(sbase), "s"(ldsb) : "memory");
}
template <int N> __device__ __forceinline__ void wait_vm() { asm volatile("s_waitcnt vmcnt(%0)" ::"i"(N) : "memory"); }
template <int N> __device__ __forceinline__ void wait_vm_bar() {
  __builtin_amdgcn_s_waitcnt(0xC07F);
  asm volatile("s_waitcnt vmcnt(%0) lgkmcnt(0)\n\ts_barrier" ::"i"(N) : "memory");
}
__device__ __forceinline__ void bar_lds() { asm volatile("s_waitcnt lgkmcnt(0)\n\ts_barrier" ::: "memory"); }
__device__ __forceinline__ bf16x8 lds_b128(lds_cptr p) { return *(const LDS_AS bf16x8*)p; }
__device__ __forceinline__ s16x4 lds_tr(lds_cptr p) { return __builtin_amdgcn_ds_read_tr16_b64_v4i16((LDS_AS s16x4*)p); }
__device__ __forceinline__ bf16x8 cat8(s16x4 a, s16x4 b) { return __builtin_shufflevector(a, b, 0, 1, 2, 3, 4, 5, 6, 7); }
__device__ __forceinline__ float xhalf_max(float v) {
  auto rr = __builtin_amdgcn_permlane32_swap(__float_as_uint(v), __float_as_uint(v), false, false);
  return fmaxf(__uint_as_float(rr[0]), __uint_as_float(rr[1]));
}
__device__ __forceinline__ float upper_half(float v) {
  auto rr = __builtin_amdgcn_permlane32_swap(__float_as_uint(v), __float_as_uint(v), false, false);
  return __uint_as_float(rr[1]);
}
__device__ __forceinline__ float gelu_tanh(float x) {
  const float c0 = -2.0f * 0.7978845608028654f * 1.4426950408889634f;
  const float c1 = c0 * 0.044715f;
  float t = fmaf(x * x, c1, c0);
  float e = __builtin_amdgcn_exp2f(x * t);
  return x * __builtin_amdgcn_rcpf(1.0f + e);
}
__device__ __forceinline__ unsigned gelu2(f32x2 x) {
  const float c0 = -2.0f * 0.7978845608028654f * 1.4426950408889634f;
  const f32x2 k0 = {c0, c0}, k1 = {c0 * 0.044715f, c0 * 0.044715f}, one = {1.0f, 1.0f};
  const f32x2 z = x * __builtin_elementwise_fma(x * x, k1, k0);
  const f32x2 d = (f32x2){__builtin_amdgcn_exp2f(z[0]), __builtin_amdgcn_exp2f(z[1])} + one;
  const f32x2 g = x * (f32x2){__builtin_amdgcn_rcpf(d[0]), __builtin_amdgcn_rcpf(d[1])};
  return cvtpk(g[0], g[1]);
}
template <int CTRL, int RMASK> __device__ __forceinline__ float dpp_add(float v) {
  return v + __uint_as_float(__builtin_amdgcn_update_dpp(0u, __float_as_uint(v), CTRL, RMASK, 0xf, false));
}
__device__ __forceinline__ float wave_sum(float v) {
  v = dpp_add<0x111, 0xf>(v); v = dpp_add<0x112, 0xf>(v); v = dpp_add<0x114, 0xf>(v); v = dpp_add<0x118, 0xf>(v);
  v = dpp_add<0x142, 0xa>(v); v = dpp_add<0x143, 0xc>(v);
  return __uint_as_float(__builtin_amdgcn_readlane(__float_as_uint(v), 63));
}
__device__ __forceinline__ u64 ld_flag(const char* ws, unsigned off) { return __hip_atomic_load((gu64*)(ws + off), RLX_AGENT); }
__device__ __forceinline__ void st_flag(char* ws, unsigned off, u64 v) { __hip_atomic_store((gu64*)(ws + off), v, RLX_AGENT); }
__device__ __forceinline__ u64 rfl64(u64 v) {
  return ((u64)(unsigned)__builtin_amdgcn_readfirstlane((unsigned)(v >> 32)) << 32) | (unsigned)__builtin_amdgcn_readfirstlane((unsigned)v);
}
constexpr unsigned SPIN_CAP = 1u << 18;
__device__ __forceinline__ void poll2(const char* ws, unsigned off1, unsigned off2, u64 epoch) {
  u64 a, b, m; unsigned c;
  asm volatile("s_mov_b32 %3, %8\n"
               ".Lpl%=:\n\t"
               "global_load_dwordx2 %0, %4, %6 sc1\n\t"
               "global_load_dwordx2 %1, %5, %6 sc1\n\t"
               "s_waitcnt vmcnt(0)\n\t"
               "v_cmp_ne_u64 %2, %7, %0\n\t"
               "v_cmp_ne_u64 vcc, %7, %1\n\t"
               "s_nop 1\n\t"
               "s_or_b64 %2, %2, vcc\n\t"
               "s_cbranch_scc0 .Lpd%=\n\t"
               "s_sleep 3\n\t"
               "s_sub_u32 %3, %3, 1\n\t"
               "s_cbranch_scc0 .Lpl%=\n"
               ".Lpd%=:"
               : "=&v"(a), "=&v"(b), "=&s"(m), "=&s"(c) : "v"(off1), "v"(off2), "s"(ws), "s"(epoch), "i"(SPIN_CAP) : "memory", "vcc", "scc");
}
__device__ __forceinline__ void lds_wait(unsigned la, unsigned want) {
  unsigned v, sv, c;
  asm volatile("s_mov_b32 %2, %5\n"
               ".Lwl%=:\n\t"
               "ds_read_b32 %0, %3\n\t"
               "s_waitcnt lgkmcnt(0)\n\t"
               "v_readfirstlane_b32 %1, %0\n\t"
               "s_nop 1\n\t"
               "s_cmp_ge_u32 %1, %4\n\t"
               "s_cbranch_scc1 .Lwd%=\n\t"
               "s_sleep 2\n\t"
               "s_sub_u32 %2, %2, 1\n\t"
               "s_cbranch_scc0 .Lwl%=\n"
               ".Lwd%=:"
               : "=&v"(v), "=&s"(sv), "=&s"(c) : "v"(la), "s"(want), "i"(SPIN_CAP * 8) : "memory", "scc");
}

constexpr int P1_SLOT = 32768, P1_W = 16384;
constexpr int P1_TOP = 4 * P1_SLOT;
constexpr int P1_B1 = P1_TOP + 24576, P1_CNT = P1_B1 + 256, P1_TCH = P1_CNT + 256, P1_CSA = P1_TCH + 256, P1_RSV = P1_CSA + 2048;
constexpr int LDS_BYTES = 163840;
static_assert(P1_RSV + 2048 <= LDS_BYTES, "lds");
constexpr unsigned OOB = 0x7ffffff0u;

__device__ __forceinline__ void glds16(unsigned voff, const void* sbase, unsigned ldsb) {
  unsigned sv;
  asm volatile("s_mov_b32 %0, m0\n\ts_mov_b32 m0, %3\n\ts_nop 0\n\tglobal_load_lds_dwordx4 %1, %2 sc1\n\ts_mov_b32 m0, %0"
               : "=&s"(sv) : "v"(voff), "s"(sbase), "s"(ldsb) : "memory");
}
__device__ __forceinline__ float addf(float a, float b) { float r; asm("v_add_f32_e32 %0, %1, %2" : "=v"(r) : "v"(a), "v"(b)); return r; }
__device__ __forceinline__ void colsum(float& lo, float& hi, const bf16x8& f) {
  const u32x4 u = __builtin_bit_cast(u32x4, f);
#pragma unroll
  for (int e = 0; e < 4; ++e) { lo = addf(lo, bflo(u[e])); hi = addf(hi, bfhi(u[e])); }
  asm volatile("" : "+v"(lo), "+v"(hi));
}
template <int E> __device__ __forceinline__ void colsum1(float& lo, float& hi, const bf16x8& f) {
  const unsigned u = __builtin_bit_cast(u32x4, f)[E];
  lo = addf(lo, bflo(u)); hi = addf(hi, bfhi(u));
  asm volatile("" : "+v"(lo), "+v"(hi));
}
__device__ __forceinline__ bf16x8 split_a(float v) {
  const unsigned h = cvtpk(v, 0.f) & 0xffffu, pk = h | (cvtpk(v - bflo(h), 0.f) << 16);
  return PK4(pk, pk, 0u, 0u);
}
__device__ __forceinline__ bf16x8 split_b(float v) {
  const unsigned h = cvtpk(v, v);
  const float l = v - bflo(h);
  return PK4(h, cvtpk(l, l), 0u, 0u);
}
struct P1Dma {
  unsigned vo[4], vm[3];
  const char *base, *wm;
  unsigned kstride;
  unsigned ldsw, lstep, ldsm;
};
template <int S, int J> __device__ __forceinline__ void p1_piece(const P1Dma& d) {
  if (S < 10) glds16(d.vo[J], d.base + S * d.kstride, d.ldsw + (S & 3) * P1_SLOT + J * d.lstep);
  else if (J < 3) glds16(d.vm[J < 3 ? J : 0], d.wm + 2 * (S - 10) * (64 * 3072), d.ldsm + (S == 14 ? P1_TOP : (S & 3) * P1_SLOT) + J * 4096);
}
template <int S> __device__ __forceinline__ void p1_issue(const P1Dma& d) { p1_piece<S, 0>(d); p1_piece<S, 1>(d); p1_piece<S, 2>(d); p1_piece<S, 3>(d); }

struct FragA { bf16x8 x0[2], x1[4], w[4]; };
struct PtrA { lds_cptr x0[4], x1[4], w[4]; LDS_AS float* csw; const char *ws, *tb; unsigned g0, g1, tv, tl; };
struct StatA { float lo, hi; u32x4 gk, go; };
template <int S, int J> __device__ __forceinline__ void rd_a(FragA& f, bf16x8 (&xk)[20], const PtrA& p) {
  constexpr int so = (S & 3) * P1_SLOT;
  if (J < 2) xk[2 * S + (J & 1)] = lds_b128(p.x0[J] + so); else f.x0[J & 1] = lds_b128(p.x0[J] + so);
  f.w[J] = cat8(lds_tr(p.w[J] + so), lds_tr(p.w[J] + so + 1024));
  f.x1[J] = lds_b128(p.x1[J] + so);
}
template <bool VT> __device__ __forceinline__ void mm_a(f32x16& acc, const bf16x8& xf, const bf16x8& wf) {
  SB(); acc = VT ? MFMA32(xf, wf, acc) : MFMA32(wf, xf, acc); SB();
}
template <bool VT, int S>
__device__ __forceinline__ void p1_stage_a(f32x16 (&acc)[2], FragA& fn, const FragA& fc, bf16x8 (&xk)[20], const PtrA& p, const P1Dma& d, StatA& st) {
  wait_vm_bar<(S == 0 ? 8 : S < 3 ? 9 : S == 3 ? 12 : S < 6 ? 11 : S == 6 ? 8 : S == 7 ? 9 : S == 8 ? 8 : 6)>();
  if (S == 9) asm volatile("" : "+v"(st.gk), "+v"(st.go));
#define CS(E, J) if (S < 9) colsum1<E>(st.lo, st.hi, fc.w[J])
  mm_a<VT>(acc[0], xk[2 * S], fc.w[0]);     if (S < 9) rd_a<(S + 1) % 10, 0>(fn, xk, p); CS(0, 0);
  mm_a<VT>(acc[1], fc.x1[0], fc.w[0]);      if (S < 9) rd_a<(S + 1) % 10, 2>(fn, xk, p); CS(1, 0);
  mm_a<VT>(acc[0], xk[2 * S + 1], fc.w[1]); p1_piece<S + 4, 0>(d); CS(2, 0);
  mm_a<VT>(acc[1], fc.x1[1], fc.w[1]);      if (S < 9) rd_a<(S + 1) % 10, 1>(fn, xk, p); CS(3, 0);
  mm_a<VT>(acc[0], fc.x0[0], fc.w[2]);      p1_piece<S + 4, 1>(d); CS(0, 2);
  mm_a<VT>(acc[1], fc.x1[2], fc.w[2]);      if (S < 9) rd_a<(S + 1) % 10, 3>(fn, xk, p); CS(1, 2);
  mm_a<VT>(acc[0], fc.x0[1], fc.w[3]);      p1_piece<S + 4, 2>(d); CS(2, 2);
  mm_a<VT>(acc[1], fc.x1[3], fc.w[3]);      p1_piece<S + 4, 3>(d); CS(3, 2);
#undef CS
  if (S == 0) glds4(p.tv, p.tb, p.tl);
  if (S == 2) p1_issue<14>(d);
  if (S == 6)
    asm volatile("global_load_dwordx4 %0, %2, %4 sc1\n\tglobal_load_dwordx4 %1, %3, %4 sc1" : "=&v"(st.gk), "=&v"(st.go) : "v"(p.g0), "v"(p.g1), "s"(p.ws) : "memory");
  if (S == 8) { colsum(st.lo, st.hi, fn.w[0]); colsum(st.lo, st.hi, fn.w[2]); st.lo += st.hi; *p.csw = st.lo; }
}
template <bool VT>
__device__ __forceinline__ void p1_pass_a(f32x16 (&acc)[2], bf16x8 (&xk)[20], const PtrA& p, const P1Dma& d, StatA& st) {
  FragA f0, f1;
  wait_vm_bar<13>();
  rd_a<0, 0>(f0, xk, p); rd_a<0, 2>(f0, xk, p); rd_a<0, 1>(f0, xk, p); rd_a<0, 3>(f0, xk, p);
  p1_stage_a<VT, 0>(acc, f1, f0, xk, p, d, st); p1_stage_a<VT, 1>(acc, f0, f1, xk, p, d, st);
  p1_stage_a<VT, 2>(acc, f1, f0, xk, p, d, st); p1_stage_a<VT, 3>(acc, f0, f1, xk, p, d, st);
  p1_stage_a<VT, 4>(acc, f1, f0, xk, p, d, st); p1_stage_a<VT, 5>(acc, f0, f1, xk, p, d, st);
  p1_stage_a<VT, 6>(acc, f1, f0, xk, p, d, st); p1_stage_a<VT, 7>(acc, f0, f1, xk, p, d, st);
  p1_stage_a<VT, 8>(acc, f1, f0, xk, p, d, st); p1_stage_a<VT, 9>(acc, f0, f1, xk, p, d, st);
}
struct FragB { bf16x8 w[2][3]; };
template <int K, int J, int T> __device__ __forceinline__ void rd_b(FragB& f, const lds_cptr (&wpb)[2][3]) {
  constexpr int slot = K >= 8 ? P1_TOP : ((10 + K / 2) & 3) * P1_SLOT;
  constexpr int off = slot + (K & 1) * 12288 + T * 64;
  f.w[J][T] = cat8(lds_tr(wpb[J][off >> 16] + (off & 0xffff)), lds_tr(wpb[J][off >> 16] + (off & 0xffff) + 768));
}
template <int K>
__device__ __forceinline__ void p1_stage_b(f32x16 (&acc)[3], FragB& fn, const FragB& fc, const bf16x8 (&xk)[20], const lds_cptr (&wpb)[2][3],
                                           float (&cb)[6], int par) {
  constexpr int N = K < 9 ? K + 1 : 0;
  mm_a<false>(acc[0], xk[2 * K], fc.w[0][0]);     if (K < 9) { rd_b<N, 0, 0>(fn, wpb); rd_b<N, 0, 1>(fn, wpb); }
  mm_a<false>(acc[1], xk[2 * K], fc.w[0][1]);     if (K < 9) rd_b<N, 0, 2>(fn, wpb);
  mm_a<false>(acc[2], xk[2 * K], fc.w[0][2]);     if (K < 9) rd_b<N, 1, 0>(fn, wpb);
  mm_a<false>(acc[0], xk[2 * K + 1], fc.w[1][0]); if (K < 9) rd_b<N, 1, 1>(fn, wpb);
  mm_a<false>(acc[1], xk[2 * K + 1], fc.w[1][1]); if (K < 9) rd_b<N, 1, 2>(fn, wpb);
  mm_a<false>(acc[2], xk[2 * K + 1], fc.w[1][2]);
  if (par == (K & 1)) { colsum(cb[0], cb[1], fc.w[0][0]); colsum(cb[2], cb[3], fc.w[0][1]); colsum(cb[4], cb[5], fc.w[0][2]); }
}
template <int N> __device__ __forceinline__ void vm_bar() { asm volatile("s_waitcnt vmcnt(%0)\n\ts_barrier" ::"i"(N) : "memory"); }

constexpr int P2_STG = 24576;
constexpr int P3_WO = 81920;
constexpr int P3_C2 = P1_CNT + 16;
template <int T, class HK>
__device__ __forceinline__ void attn_step(HK&& hook, f32x16& Sc, f32x16& Sn, f32x16& o, const bf16x8& q0, const bf16x8& q1, bf16x8& k0, bf16x8& k1,
                                          bf16x8& v0, bf16x8& v1, unsigned (&p)[8], float (&e)[4], lds_cptr kc, lds_cptr kn, lds_cptr vc,
                                          lds_cptr vn, int k1off) {
  const f32x16 zero = {};
  const lds_cptr k2p = (T + 2 < 8) ? kc + (T + 2) * 3072 : kn + (T + 2 - 8) * 3072;
  const lds_cptr v1p = (T + 1 < 8) ? vc + (T + 1) * 3072 : vn;
  float a0, a1, a2, a3;
  Sn = MFMA32(k0, q0, zero);
  k0 = lds_b128(k2p);
  hook(2 * T);
  SB();
  p[6] = cvtpk(e[0], e[1]); p[7] = cvtpk(e[2], e[3]);
  SB();
  a0 = EXP2(Sc[0]); a1 = EXP2(Sc[1]); a2 = EXP2(Sc[2]); a3 = EXP2(Sc[3]);
  SB();
  o = MFMA32(v1, PK4(p[4], p[5], p[6], p[7]), o);
  v1 = lds_b128(vc + T * 3072 + 768);
  SB();
  p[0] = cvtpk(a0, a1); p[1] = cvtpk(a2, a3);
  SB();
  a0 = EXP2(Sc[4]); a1 = EXP2(Sc[5]); a2 = EXP2(Sc[6]); a3 = EXP2(Sc[7]);
  SB();
  Sn = MFMA32(k1, q1, Sn);
  k1 = lds_b128(k2p + k1off);
  hook(2 * T + 1);
  SB();
  p[2] = cvtpk(a0, a1); p[3] = cvtpk(a2, a3);
  SB();
  a0 = EXP2(Sc[8]); a1 = EXP2(Sc[9]); a2 = EXP2(Sc[10]); a3 = EXP2(Sc[11]);
  SB();
  o = MFMA32(v0, PK4(p[0], p[1], p[2], p[3]), o);
  v0 = lds_b128(v1p);
  SB();
  p[4] = cvtpk(a0, a1); p[5] = cvtpk(a2, a3);
  SB();
  e[0] = EXP2(Sc[12]); e[1] = EXP2(Sc[13]); e[2] = EXP2(Sc[14]); e[3] = EXP2(Sc[15]);
  SB();
}

__device__ __forceinline__ void setprio_rt(int p) {
  if (p == 0) __builtin_amdgcn_s_setprio(0); else if (p == 1) __builtin_amdgcn_s_setprio(1); else if (p == 2) __builtin_amdgcn_s_setprio(2); else __builtin_amdgcn_s_setprio(3);
}
__device__ __forceinline__ int v_colblock(int tile) { return tile < 48 ? tile >> 2 : 12 + (tile - 48) / 3; }

__global__ __launch_bounds__(512, 2) void mega_kernel(const u16* __restrict__ x, const u16* __restrict__ wqkv, const u16* __restrict__ wo,
                                                       const u16* __restrict__ w1, const u16* __restrict__ b1, const u16* __restrict__ w2,
                                                       const u16* __restrict__ b2, char* ws, u16* __restrict__ out) {
  __shared__ __attribute__((aligned(16))) char lds[LDS_BYTES];
  const unsigned lds0 = (unsigned)(uintptr_t)(LDS_AS char*)lds;
  const int b = blockIdx.x, xcd = b & 7, i = b >> 3;
  const int tid = threadIdx.x, lane = tid & 63, w = __builtin_amdgcn_readfirstlane(tid >> 6);
  const int r = lane & 31, hh = lane >> 5, tt = w & 3;
  const auto wsr = __builtin_amdgcn_make_buffer_rsrc(ws, 0, WS_END, 0x00020000);
  STAMP(0);
  const int head = xcd * 4 + (i & 3), qb = i >> 2;
  const int p = (xcd & 1) * 8 + (i & 7), cb = (xcd >> 1) * 4 + (i >> 3);
  const int id1 = p * 16 + cb;
  u64 epoch_v; u32x4 xa, xb;
  {
    const unsigned xo = (unsigned)((p * 128 + cb * 8 + w) * 1280);
    asm volatile("global_load_dwordx2 %0, %3, %6 sc1\n\tglobal_load_dwordx4 %1, %4, %7\n\tglobal_load_dwordx4 %2, %5, %7"
                 : "=&v"(epoch_v), "=&v"(xa), "=&v"(xb) : "v"(WS_SYNC), "v"(xo + lane * 16), "v"(xo + 1024 + (lane & 15) * 16), "s"(ws), "s"(x) : "memory");
  }
  {
    u64 pc; asm volatile("s_getpc_b64 %0" : "=s"(pc));
    if (w == 6) glds4((unsigned)min((i * 14 + min(lane, 13)) * 128, PFC), (const char*)(pc & ~255ull), lds0 + P1_TCH);
  }
  if (tid < 8) ((LDS_AS unsigned*)(LDS_AS char*)&lds[P1_CNT])[tid] = 0u;
  u64 epoch;
  const char* kvg = ws + WS_KV + head * 196608;
  const unsigned vdma = (unsigned)(w * 3072 + lane * 16);
  const unsigned qo = WS_Q + (head * 64 + qb * 8 + w) * 1536;
  bf16x8 q0; u32x4 q1u;

  {
    const int qt0 = cb < 12 ? cb * 4 : 48 + (cb - 12) * 3;
    const int nq = cb < 12 ? 4 : 3;
    const int wh = w >> 2, nt = w >> 1, tp = w & 1;
    const int tk = 2 * tp + (nt & 1), to = tk ^ 1;
    P1Dma d;
    if (w >= 4) {
      const int qr = 4 * tt + (lane >> 4);
#pragma unroll
      for (int q = 0; q < 4; ++q)
        d.vo[q] = (unsigned)((qr + 16 * q) * 3840 + (qt0 + min((((lane >> 2) & 3) ^ (qr & 3)), nq - 1)) * 64 + (lane & 3) * 16);
      d.base = (const char*)wqkv; d.kstride = 64 * 3840; d.ldsw = lds0 + P1_W + tt * 1024; d.lstep = 4096;
    } else {
#pragma unroll
      for (int q = 0; q < 4; ++q) {
        const int row = 32 * tt + 8 * q + (lane >> 3);
        d.vo[q] = (unsigned)((p * 128 + row) * 1280 + (((lane & 7) ^ ((row >> 1) & 7)) << 4));
      }
      d.base = (const char*)x; d.kstride = 128; d.ldsw = lds0 + tt * 4096; d.lstep = 1024;
    }
    if (w == 7) glds4((unsigned)(cb * 192 + min(lane, 47) * 4), b1, lds0 + P1_B1);
    p1_issue<0>(d);
    d.wm = (const char*)w1 + wh * (64 * 3072); d.ldsm = lds0 + wh * 12288 + tt * 1024;
#pragma unroll
    for (int q = 0; q < 3; ++q) { const int u = q * 256 + tt * 64 + lane; d.vm[q] = (unsigned)((u / 12) * 3072 + cb * 192 + (u % 12) * 16); }
    asm volatile("s_waitcnt vmcnt(" PFW ")" : "+v"(epoch_v), "+v"(xa), "+v"(xb)::"memory");
    epoch = rfl64(epoch_v) + 1;
    const unsigned tag = (unsigned)epoch ^ (unsigned)(epoch >> 32);
    {
      const float c = __uint_as_float(__builtin_amdgcn_readfirstlane(xa[0]) << 16);
      float s1 = 0.f, s2 = 0.f, t1 = 0.f, t2 = 0.f;
#pragma unroll
      for (int e = 0; e < 4; ++e) {
        const float a0 = bflo(xa[e]) - c, a1 = bfhi(xa[e]) - c, b0 = bflo(xb[e]) - c, b1v = bfhi(xb[e]) - c;
        s1 += a0 + a1; s2 = fmaf(a0, a0, fmaf(a1, a1, s2)); t1 += b0 + b1v; t2 = fmaf(b0, b0, fmaf(b1v, b1v, t2));
      }
      if (lane < 16) { s1 += t1; s2 += t2; }
      const float m = wave_sum(s1) * (1.0f / 640.0f);
      const float var = fmaxf(wave_sum(s2) * (1.0f / 640.0f) - m * m, 0.0f);
      const float r1 = __builtin_amdgcn_rsqf(var + EPS);
      const float r2 = __builtin_amdgcn_rsqf(var * r1 * r1 + EPS);
      ST16(((u32x4){__float_as_uint(m + c), __float_as_uint(r1), __float_as_uint(r1 * r2), tag}), lane == 0 ? WS_RS + (p * 128 + cb * 8 + w) * 16 : OOB);
    }
    STAMP(1);
    p1_issue<1>(d); p1_issue<2>(d); p1_issue<3>(d);
    PtrA pa;
    {
      if (w < 3) {
        const int v = w * 64 + lane;
        pa.tv = (unsigned)((p * 128 + (i >> 3) * 32 + min(v / 5, 31)) * 1280 + 640 + (v % 5) * 128); pa.tb = (const char*)x;
      } else if (w < 5) {
        const int v = (w - 3) * 64 + lane;
        pa.tv = (unsigned)((320 + (i & 7) * 40 + min(v / 3, 39)) * 3840 + min(qt0 * 64 + (v % 3) * 124, 3836)); pa.tb = (const char*)wqkv;
      } else {
        const int v = (w - 5) * 64 + lane;
        pa.tv = (unsigned)(((i & 7) * 80 + min(v >> 1, 79)) * 3072 + cb * 192 + (v & 1) * 188); pa.tb = (const char*)w1;
      }
      pa.tl = lds0 + P1_TCH;
    }
    {
      const int q4 = (lane & 15) >> 2;
      const lds_cptr wb = (lds_cptr)lds + P1_W + (8 * hh + q4) * 256 + (nt ^ q4) * 64 + ((lane >> 4) & 1) * 32 + (lane & 3) * 8;
#pragma unroll
      for (int j = 0; j < 4; ++j) {
        const int ks = 2 * ((j >> 1) ^ wh) + ((j & 1) ^ tp);
        const int cs = ((2 * ks + hh) ^ ((r >> 1) & 7)) << 4;
        pa.x0[j] = (lds_cptr)lds + (tk * 32 + r) * 128 + cs;
        pa.x1[j] = (lds_cptr)lds + (to * 32 + r) * 128 + cs;
        pa.w[j] = wb + ks * 4096;
      }
      pa.csw = (LDS_AS float*)(LDS_AS char*)&lds[P1_CSA] + w * 64 + lane;
      pa.ws = ws; pa.g0 = WS_RS + (p * 128 + tk * 32 + r) * 16; pa.g1 = WS_RS + (p * 128 + to * 32 + r) * 16;
    }
    lds_cptr wpb[2][3];
#pragma unroll
    for (int j = 0; j < 2; ++j) {
      wpb[j][0] = (lds_cptr)lds + (32 * wh + 16 * (j ^ tp) + 8 * hh + ((lane & 15) >> 2)) * 192 + (((lane >> 4) & 1) ^ wh) * 32 + (lane & 3) * 8;
      wpb[j][1] = wpb[j][0] + 65536; wpb[j][2] = wpb[j][0] + 131072;
    }
    bf16x8 xk[20];
    FragB g0, g1;
    StatA st;
    st.lo = 0.f; st.hi = 0.f;
    {
      f32x16 acc[2] = {};
      if (cb >= 10) p1_pass_a<true>(acc, xk, pa, d, st);
      else p1_pass_a<false>(acc, xk, pa, d, st);
      STAMP(3);
      const float csp = ((LDS_AS float*)(LDS_AS char*)&lds[P1_CSA])[(w ^ 1) * 64 + lane];
      rd_b<0, 0, 0>(g0, wpb); rd_b<0, 0, 1>(g0, wpb); rd_b<0, 0, 2>(g0, wpb); rd_b<0, 1, 0>(g0, wpb); rd_b<0, 1, 1>(g0, wpb); rd_b<0, 1, 2>(g0, wpb);
      SB();
      if (__builtin_expect(!__all(st.gk[3] == tag && st.go[3] == tag), 0)) {
#pragma nounroll
        for (unsigned sp = 0; sp < SPIN_CAP; ++sp) {
          st.gk = __builtin_bit_cast(u32x4, LD16(pa.g0, 0)); st.go = __builtin_bit_cast(u32x4, LD16(pa.g1, 0));
          if (__all(st.gk[3] == tag && st.go[3] == tag)) break;
          __builtin_amdgcn_s_sleep(3);
          asm volatile("" ::: "memory");
        }
      }
      const bf16x8 csf = split_a(tp ? csp + st.lo : st.lo + csp);
      const bf16x8 mk = split_b(-__uint_as_float(st.gk[0])), mo = split_b(-__uint_as_float(st.go[0]));
      if (cb >= 10) {
        acc[0] = MFMA32(mk, csf, acc[0]); acc[1] = MFMA32(mo, csf, acc[1]);
        LDS_AS float* rsv = (LDS_AS float*)(LDS_AS char*)&lds[P1_RSV] + w * 64;
        rsv[lane] = __uint_as_float(hh ? st.go[1] : st.gk[1]);
        const int n = (qt0 + nt) * 32 + r - 1280;
        const int hd = n / 20, dd = n - hd * 20;
        const unsigned ov = dd == 18 ? 0x3f803f80u : 0u;
#pragma unroll
        for (int t = 0; t < 2; ++t) {
          unsigned pk[8];
#pragma unroll
          for (int g = 0; g < 4; ++g) {
            const f32x4 rr = *(const LDS_AS f32x4*)(rsv + t * 32 + 8 * g + 4 * hh);
            pk[2 * g] = cvtpk(acc[t][4 * g] * rr[0], acc[t][4 * g + 1] * rr[1]); pk[2 * g + 1] = cvtpk(acc[t][4 * g + 2] * rr[2], acc[t][4 * g + 3] * rr[3]);
          }
          const unsigned vp = nt < nq ? WS_KV + hd * 196608 + (p * 4 + (t ? to : tk)) * 3072 + 1536 + (hh * 24 + dd) * 16 : OOB;
          ST16(((u32x4){pk[0], pk[1], pk[2], pk[3]}), vp);
          ST16(((u32x4){pk[4], pk[5], pk[6], pk[7]}), vp + 768);
          const unsigned vq = dd >= 18 ? vp + 32 : OOB;
          ST16(((u32x4){ov, ov, ov, ov}), vq);
          ST16(((u32x4){ov, ov, ov, ov}), vq + 768);
        }
      } else {
        acc[0] = MFMA32(csf, mk, acc[0]); acc[1] = MFMA32(csf, mo, acc[1]);
        const bool isq = cb < 5;
        const float qs = isq ? QSCALE : 1.0f;
        const float rs2[2] = {qs * __uint_as_float(st.gk[1]), qs * __uint_as_float(st.go[1])};
        const unsigned ts = isq ? 1536u : 3072u;
        const unsigned base = (isq ? WS_Q : WS_KV) + p * 4 * ts + r * 16;
        const unsigned hs = isq ? 64u * 1536u : 196608u;
        const unsigned cst = isq ? 0u : 0x3f80u;
#pragma unroll
        for (int g = 0; g < 4; g += 2) {
          const unsigned os = (qt0 + nt) * 4 + g + hh - (isq ? 0 : 80);
          const unsigned hp = os / 5u, oi = os - hp * 5u;
          const bool sp = oi == 2;
          const unsigned a0 = base + (2 * hp + (oi > 2)) * hs + (oi > 2 ? oi - 3 : oi) * 512;
#pragma unroll
          for (int t = 0; t < 2; ++t) {
            const float rs = rs2[t];
            const auto s0 = __builtin_amdgcn_permlane32_swap(cvtpk(acc[t][4 * g] * rs, acc[t][4 * g + 1] * rs),
                                                             cvtpk(acc[t][4 * g + 4] * rs, acc[t][4 * g + 5] * rs), false, false);
            const auto s1 = __builtin_amdgcn_permlane32_swap(cvtpk(acc[t][4 * g + 2] * rs, acc[t][4 * g + 3] * rs),
                                                             cvtpk(acc[t][4 * g + 6] * rs, acc[t][4 * g + 7] * rs), false, false);
            const unsigned a1 = a0 + (t ? to : tk) * ts;
            ST16(((u32x4){s0[0], s1[0], sp ? cst : s0[1], sp ? 0u : s1[1]}), a1);
            ST16(((u32x4){s0[1], s1[1], cst, 0u}), sp ? a1 + hs : OOB);
          }
        }
      }
      SB();
    }
    STAMP(16);
    const float mean_k = __uint_as_float(st.gk[0]), rs = __uint_as_float(st.gk[2]);
    f32x16 acc[3] = {};
    float cbs[6] = {};
    const int par = (nt & 1) ^ wh;
    p1_stage_b<0>(acc, g1, g0, xk, wpb, cbs, par);
    vm_bar<14>();
    const int bk = w * 256 + b;
    const int rto = w == 0 ? 24576 : w == 1 ? 57344 : 65536;
    if (bk < 544) {
      const int kk = bk % 68, c8 = bk / 68;
      const int r0 = kk < 48 ? 32 * kk : kk < 64 ? 40 * (kk - 48) : 160 * (kk - 64) + 16;
      const int ra = kk < 48 ? 16 : kk < 64 ? 20 : 80, rbb = kk < 64 ? 8 : 20, rc = kk < 64 ? 4 : 40;
#pragma unroll
      for (int q = 0; q < 5; ++q) {
        const int u = q * 64 + lane, rho = u / 10;
        const int row = r0 + ra * (rho >> 4) + rc * ((rho >> 3) & 1) + rbb * ((rho >> 2) & 1) + (rho & 3);
        glds16((unsigned)(row * 1280 + c8 * 160 + (u % 10) * 16), kk < 48 ? w2 : wo, lds0 + rto + q * 1024);
      }
    }
    p1_stage_b<1>(acc, g0, g1, xk, wpb, cbs, par); p1_stage_b<2>(acc, g1, g0, xk, wpb, cbs, par);
    if (bk < 544) vm_bar<16>(); else vm_bar<11>();
    STAMP(17);
    p1_stage_b<3>(acc, g0, g1, xk, wpb, cbs, par); p1_stage_b<4>(acc, g1, g0, xk, wpb, cbs, par);
    STAMP(18);
    if (bk < 544) vm_bar<5>(); else vm_bar<0>();
    STAMP(19);
    if (tid == 0) st_flag(ws, WS_F1 + id1 * 128, epoch);
    p1_stage_b<5>(acc, g0, g1, xk, wpb, cbs, par); p1_stage_b<6>(acc, g1, g0, xk, wpb, cbs, par);
    p1_stage_b<7>(acc, g0, g1, xk, wpb, cbs, par);
    if (bk < 544) {
      wait_vm<0>();
      const lds_cptr rp = (lds_cptr)lds + rto + (8 * (lane >> 4) + ((lane & 15) >> 2)) * 160 + (lane & 3) * 8;
#pragma unroll
      for (int t = 0; t < 5; ++t)
        ST16(__builtin_bit_cast(u32x4, cat8(lds_tr(rp + t * 32), lds_tr(rp + t * 32 + 640))), WS_WT + (bk * 5 + t) * 1024 + lane * 16);
    }
    unsigned fo1 = 0, fo2 = 0;
    u64 fa = 0, fb = 0;
    if (w == 3) {
      const int ca = (head * 20) >> 7, cbb = (head * 20 + 19) >> 7;
      const int va = v_colblock(40 + ((head * 20) >> 5)), vb = v_colblock(40 + ((head * 20 + 19) >> 5));
      const int sel = lane >> 4;
      const int c1 = sel == 0 ? 5 + ca : sel == 1 ? 5 + cbb : sel == 2 ? va : vb;
      fo1 = WS_F1 + ((lane & 15) * 16 + c1) * 128;
      fo2 = WS_F1 + ((2 * qb + (lane & 1)) * 16 + ((lane & 2) ? cbb : ca)) * 128;
      fa = ld_flag(ws, fo1); fb = ld_flag(ws, fo2);
    }
    p1_stage_b<8>(acc, g1, g0, xk, wpb, cbs, par);
    p1_stage_b<9>(acc, g0, g1, xk, wpb, cbs, par);
    STAMP(4);
    LDS_AS f32x4* xch = (LDS_AS f32x4*)(LDS_AS char*)&lds[73728];
    LDS_AS float* cst = (LDS_AS float*)(LDS_AS char*)&lds[73728 + 49152];
    const int rsw = lane ^ (wh << 4);
#pragma unroll
    for (int t = 0; t < 3; ++t) {
      cst[(t * 64 + rsw) * 8 + w] = cbs[2 * t] + cbs[2 * t + 1];
#pragma unroll
      for (int u = 0; u < 2; ++u)
        xch[(w * 6 + 2 * t + u) * 64 + lane] = (f32x4){acc[t][8 + 4 * u], acc[t][9 + 4 * u], acc[t][10 + 4 * u], acc[t][11 + 4 * u]};
    }
    if (w == 3) { SB(); if (!__all(fa == epoch && fb == epoch)) poll2(ws, fo1, fo2, epoch); SB(); }
    bar_lds();
    STAMP(20);
    f32x4 c0[3], c1[3], px[6];
    u32x2 bb[6];
#pragma unroll
    for (int t = 0; t < 3; ++t) { c0[t] = *(const LDS_AS f32x4*)(cst + (t * 64 + rsw) * 8); c1[t] = *(const LDS_AS f32x4*)(cst + (t * 64 + rsw) * 8 + 4); }
#pragma unroll
    for (int q = 0; q < 6; ++q) {
      px[q] = xch[((w ^ 4) * 6 + q) * 64 + lane];
      bb[q] = *(const u32x2*)(lds + P1_B1 + ((q >> 1) * 32 + 16 * wh + 8 * (q & 1) + 4 * hh) * 2);
    }
    SB();
    glds16x3(vdma, kvg, lds0 + w * 3072);
    q0 = LD16(qo + lane * 16, 0);
    q1u = __builtin_bit_cast(u32x4, LD16(qo + 1024 + r * 16, 0));
    SB();
    glds16x3(vdma, kvg + P2_STG, lds0 + P2_STG + w * 3072);
    glds16x3(vdma, kvg + 2 * P2_STG, lds0 + 2 * P2_STG + w * 3072);
    SB();
    const unsigned hp = WS_H + ((p * 4 + tk) * 96 + cb * 6 + wh) * 1024 + lane * 16;
    const bf16x8 mk = split_b(-mean_k);
#pragma unroll
    for (int t = 0; t < 3; ++t) {
      const f32x4 cc = c0[t] + c1[t];
      acc[t] = MFMA32(split_a((cc[0] + cc[1]) + (cc[2] + cc[3])), mk, acc[t]);
    }
#pragma unroll
    for (int t = 0; t < 3; ++t) {
      unsigned pk[4];
#pragma unroll
      for (int u = 0; u < 2; ++u) {
        const f32x4 a = (f32x4){acc[t][4 * u], acc[t][4 * u + 1], acc[t][4 * u + 2], acc[t][4 * u + 3]} + px[2 * t + u];
        const u32x2 bq = bb[2 * t + u];
        pk[2 * u] = gelu2((f32x2){a[0], a[1]} * rs + (f32x2){bflo(bq[0]), bfhi(bq[0])});
        pk[2 * u + 1] = gelu2((f32x2){a[2], a[3]} * rs + (f32x2){bflo(bq[1]), bfhi(bq[1])});
      }
      ST16(((u32x4){pk[0], pk[1], pk[2], pk[3]}), hp + t * 2048);
    }
    STAMP(21);
  }

  const int rb = xcd * 4 + (i & 3), cq = i >> 2;
  const unsigned ao = (lane >> 5) * 1024 + (((lane >> 4) & 1) * 32 + (lane & 15)) * 16;
  const unsigned wofs = WS_WT + cq * 68 * 5 * 1024 + lane * 16;
  bf16x8 af[3][4], wf[3][5];
  auto ldha = [&](int s, int kk) {
#pragma unroll
    for (int m = 0; m < 4; ++m) af[s][m] = LD16(ao + (m & 1) * 256, WS_H + ((2 * rb + (m >> 1)) * 96 + 2 * kk) * 1024);
  };
  auto ldw = [&](int s, int kk) {
#pragma unroll
    for (int t = 0; t < 5; ++t) wf[s][t] = LD16(wofs + t * 1024, kk * 5120);
  };
  auto ldh = [&](int s, int kk) { ldha(s, kk); ldw(s, kk); };
  const int kh0 = w < 4 ? 9 * w : 36 + 3 * (w - 4);
  auto wo_piece = [&](int q) {
    const int n = min(w * 8 + q, 59), j = n / 15;
    glds16(wofs, ws + (48 + 5 * j) * 5120 + (n - 15 * j) * 1024, lds0 + P3_WO + n * 1024);
  };
  {
    if (hh) q1u = (u32x4){0u, 0u, 0u, 0u};
    const int dcl = r < 21 ? r : 21;
    const int k1off = hh ? 1536 + 21 * 16 - lane * 16 : 1024;
    const lds_cptr kb = (lds_cptr)lds + lane * 16;
    const lds_cptr vb = (lds_cptr)lds + 1536 + (hh * 24 + dcl) * 16;
    const f32x16 zero = {};
    wait_vm_bar<6>();
    STAMP(22);
    bf16x8 k0 = lds_b128(kb), k1 = lds_b128(kb + k1off);
    f32x16 sa = MFMA32(k0, q0, zero), sb;
    {
      f32x16 s = MFMA32(k1, __builtin_bit_cast(bf16x8, q1u), sa);
      float m = s[0];
#pragma unroll
      for (int q = 1; q < 16; ++q) m = fmaxf(m, s[q]);
      m = xhalf_max(m);
      q1u[2] = hh ? 0u : (cvtpk(-m, 0.0f) & 0xffffu);
    }
    const bf16x8 q1 = __builtin_bit_cast(bf16x8, q1u);
    sa = MFMA32(k1, q1, sa);
    k0 = lds_b128(kb + 3072); k1 = lds_b128(kb + 3072 + k1off);
    bf16x8 v0 = lds_b128(vb), v1 = {};
    unsigned pp[8] = {};
    float e[4] = {};
    f32x16 o = {};
    int sc = 0;
    u64 fl = 0;
    const unsigned flo = WS_F1B + (w * 32 + r) * 128;
    __builtin_amdgcn_s_setprio(2);
    STAMP(6);
#define ATTN_STAGE(HOOK) do { \
      const int sn = sc == 2 * P2_STG ? 0 : sc + P2_STG; \
      const lds_cptr kc = kb + sc, kn = kb + sn, vc = vb + sc, vn = vb + sn; \
      attn_step<0>(HOOK, sa, sb, o, q0, q1, k0, k1, v0, v1, pp, e, kc, kn, vc, vn, k1off); \
      attn_step<1>(HOOK, sb, sa, o, q0, q1, k0, k1, v0, v1, pp, e, kc, kn, vc, vn, k1off); \
      attn_step<2>(HOOK, sa, sb, o, q0, q1, k0, k1, v0, v1, pp, e, kc, kn, vc, vn, k1off); \
      attn_step<3>(HOOK, sb, sa, o, q0, q1, k0, k1, v0, v1, pp, e, kc, kn, vc, vn, k1off); \
      attn_step<4>(HOOK, sa, sb, o, q0, q1, k0, k1, v0, v1, pp, e, kc, kn, vc, vn, k1off); \
      attn_step<5>(HOOK, sb, sa, o, q0, q1, k0, k1, v0, v1, pp, e, kc, kn, vc, vn, k1off); \
      attn_step<6>(HOOK, sa, sb, o, q0, q1, k0, k1, v0, v1, pp, e, kc, kn, vc, vn, k1off); \
      attn_step<7>(HOOK, sb, sa, o, q0, q1, k0, k1, v0, v1, pp, e, kc, kn, vc, vn, k1off); \
      scp = sc; sc = sn; } while (0)
    int scp;
#pragma nounroll
    for (int st = 0; st < 6; ++st) {
      ATTN_STAGE([](int n) { if ((n & 3) == 0) setprio_rt(3 - (n >> 2)); });
      if (st == 5 && !__all(fl == epoch)) poll2(ws, flo, flo, epoch);
      asm volatile("s_waitcnt vmcnt(0) lgkmcnt(2)\n\ts_barrier" ::: "memory");
      if (st < 5) glds16x3(vdma, kvg + (st + 3) * P2_STG, lds0 + scp + w * 3072);
      if (st == 4) fl = ld_flag(ws, flo);
      if (st == 0 && tid == 0) st_flag(ws, WS_F1B + b * 128, epoch);
      SB();
    }
    auto pf = [&](int n) {
      if (n < 27) {
        const int s = n / 9, j = n % 9, kk = kh0 + s;
        if (j < 4) af[s][j] = LD16(ao + (j & 1) * 256, WS_H + ((2 * rb + (j >> 1)) * 96 + 2 * kk) * 1024);
        else wf[s][j - 4] = LD16(wofs + (j - 4) * 1024, kk * 5120);
      } else {
        wo_piece(n - 24);
      }
    };
    wo_piece(0); wo_piece(1); wo_piece(2);
    ATTN_STAGE([&](int n) { pf(n); if (n == 0) setprio_rt(3); if (n == 11) setprio_rt(2); });
    ATTN_STAGE([&](int n) { pf(n + 16); if (n == 6) setprio_rt(1); });
    STAMP(7);
    pp[6] = cvtpk(e[0], e[1]); pp[7] = cvtpk(e[2], e[3]);
    o = MFMA32(v1, PK4(pp[4], pp[5], pp[6], pp[7]), o);
    float l = upper_half(o[8]);
    float amax = 0.f;
#pragma unroll
    for (int q = 0; q < 12; ++q) amax = fmaxf(amax, fabsf(o[q]));
    const bool bad = !(l < 1.0e9f) || !(l > 0.25f) || !(amax < 1.0e30f);
    if (__builtin_expect(__any(bad), 0)) {
      q1u[2] = 0u;
      const bf16x8 q1z = __builtin_bit_cast(bf16x8, q1u);
      float m = -INFINITY;
      o = zero;
#pragma nounroll
      for (int t = 0; t < 64; ++t) {
        const unsigned tp = WS_KV + head * 196608 + t * 3072;
        f32x16 s = MFMA32(LD16(tp + lane * 16, 0), q0, zero);
        s = MFMA32(LD16(tp + k1off + lane * 16, 0), q1z, s);
        float tm = s[0];
#pragma unroll
        for (int q = 1; q < 16; ++q) tm = fmaxf(tm, s[q]);
        tm = xhalf_max(tm);
        const float mn = fmaxf(m, tm);
        const float al = EXP2(m - mn);
        m = mn;
        unsigned pk[8];
#pragma unroll
        for (int q = 0; q < 8; ++q) pk[q] = cvtpk(EXP2(s[2 * q] - mn), EXP2(s[2 * q + 1] - mn));
#pragma unroll
        for (int q = 0; q < 16; ++q) o[q] *= al;
        const unsigned vp = tp + 1536 + (hh * 24 + dcl) * 16;
        o = MFMA32(LD16(vp, 0), PK4(pk[0], pk[1], pk[2], pk[3]), o);
        o = MFMA32(LD16(vp + 768, 0), PK4(pk[4], pk[5], pk[6], pk[7]), o);
      }
      l = upper_half(o[8]);
    }
    const float il = 1.0f / l;
    const unsigned op = WS_O + (qb * 8 + w) * 40 * 1024;
    ST16(((u32x4){cvtpk(o[0] * il, o[1] * il), cvtpk(o[2] * il, o[3] * il), cvtpk(o[4] * il, o[5] * il), cvtpk(o[6] * il, o[7] * il)}),
         op + head * 1024 + lane * 16);
    ST8(((u32x2){cvtpk(o[8] * il, o[9] * il), cvtpk(o[10] * il, o[11] * il)}),
        hh ? OOB : op + (32 + (head >> 2)) * 1024 + (((head >> 1) & 1) * 32 + r) * 16 + (head & 1) * 8);
    STAMP(8);
  }

  {
    const int g = lane >> 4, c = lane & 15;
    f32x4 acc[4][5] = {};
    LDS_AS f32x4* red = (LDS_AS f32x4*)(LDS_AS char*)lds;
    LDS_AS unsigned* cnt = (LDS_AS unsigned*)(LDS_AS char*)&lds[P1_CNT];
    auto mm = [&](int s) {
      SB();
#pragma unroll
      for (int m = 0; m < 4; ++m)
#pragma unroll
        for (int t = 0; t < 5; ++t) acc[m][t] = MFMA16(wf[s][t], af[s][m], acc[m][t]);
      SB();
    };
    wait_vm<0>();
    {
      unsigned old = 0;
      if (lane == 0) old = __hip_atomic_fetch_add(cnt, 1u, __ATOMIC_RELAXED, __HIP_MEMORY_SCOPE_WORKGROUP);
      if (lane == 0 && old == 7) {
        st_flag(ws, WS_F2 + (qb * 32 + head) * 128, epoch);
        if (b == 0) st_flag(ws, WS_SYNC, epoch);
      }
    }
    STAMP(13);
    __builtin_amdgcn_s_setprio(0);
    mm(0);
    u32x2 xv[3] = {}, bv[3] = {};
    auto ldres = [&]() {
#pragma unroll
      for (int u = 0; u < 3; ++u) {
        const int tl = min(w + 8 * u, 19), m = tl / 5, t = tl - m * 5;
        xv[u] = *(const u32x2*)(x + (size_t)(rb * 64 + m * 16 + c) * 640 + cq * 80 + t * 16 + 4 * g);
        bv[u] = *(const u32x2*)(b2 + cq * 80 + t * 16 + 4 * g);
      }
    };
    const int j = w & 3;
    ldw(0, w < 4 ? kh0 + 3 : 48 + 5 * j + 3);
    if (w < 4) ldha(0, kh0 + 3);
    mm(1);
    ldw(1, w < 4 ? kh0 + 4 : 48 + 5 * j + 4);
    if (w < 4) ldha(1, kh0 + 4);
    mm(2);
    if (w < 4) {
      ldh(2, kh0 + 5);
      mm(0); ldh(0, kh0 + 6);
      mm(1); ldh(1, kh0 + 7);
      mm(2); ldh(2, kh0 + 8);
      mm(0); ldres();
      mm(1); mm(2);
      STAMP(14);
      lds_wait(lds0 + P1_CNT, 8u);
#pragma unroll
      for (int m = 0; m < 4; ++m)
#pragma unroll
        for (int t = 0; t < 5; ++t) red[(w * 20 + m * 5 + t) * 64 + lane] = acc[m][t];
      asm volatile("s_waitcnt lgkmcnt(0)" ::: "memory");
      __hip_atomic_store(cnt + 4 + w, 1u, __ATOMIC_RELAXED, __HIP_MEMORY_SCOPE_WORKGROUP);
      STAMP(10);
    }
    int w2 = w;
    asm volatile("" : "+s"(w2));
    if (w2 >= 4) {
      STAMP(14);
      STAMP(15);
      {
        const unsigned fo = WS_F2 + ((rb >> 2) * 32 + (lane & 31)) * 128;
        poll2(ws, fo, fo, epoch);
      }
      STAMP(9);
      bf16x8 of[5][4];
#pragma unroll
      for (int k = 0; k < 5; ++k)
#pragma unroll
        for (int m = 0; m < 4; ++m) of[k][m] = LD16(ao + (m & 1) * 256, WS_O + ((2 * rb + (m >> 1)) * 40 + 2 * (5 * j + (k + 3) % 5)) * 1024);
      SB();
      lds_wait(lds0 + P1_CNT, 8u);
      const lds_cptr wol = (lds_cptr)lds + P3_WO + j * 15360 + lane * 16;
#pragma unroll
      for (int k = 0; k < 5; ++k) {
        if (k >= 1 && k < 4) {
#pragma unroll
          for (int t = 0; t < 5; ++t) wf[(k + 1) & 1][t] = lds_b128(wol + ((k - 1) * 5 + t) * 1024);
        }
        if (k == 2) ldres();
        SB();
#pragma unroll
        for (int m = 0; m < 4; ++m)
#pragma unroll
          for (int t = 0; t < 5; ++t) acc[m][t] = MFMA16(wf[k & 1][t], of[k][m], acc[m][t]);
        SB();
      }
      STAMP(10);
      lds_wait(lds0 + P1_CNT + 16 + 4 * j, 1u);
#pragma unroll
      for (int m = 0; m < 4; ++m) {
#pragma unroll
        for (int t = 0; t < 5; ++t) red[(j * 20 + m * 5 + t) * 64 + lane] += acc[m][t];
      }
    }
    bar_lds();
    STAMP(11);
#pragma unroll
    for (int u = 0; u < 3; ++u) {
      const int tl = w + 8 * u;
      if (tl < 20) {
        const int m = tl / 5, t = tl - m * 5;
        f32x4 a = red[tl * 64 + lane];
#pragma unroll
        for (int q = 1; q < 4; ++q) a += red[(q * 20 + tl) * 64 + lane];
        *(u32x2*)(out + (size_t)(rb * 64 + m * 16 + c) * 640 + cq * 80 + t * 16 + 4 * g) =
            (u32x2){cvtpk(a[0] + bflo(xv[u][0]) + bflo(bv[u][0]), a[1] + bfhi(xv[u][0]) + bfhi(bv[u][0])),
                    cvtpk(a[2] + bflo(xv[u][1]) + bflo(bv[u][1]), a[3] + bfhi(xv[u][1]) + bfhi(bv[u][1]))};
      }
    }
    STAMP(12);
  }
}

extern "C" void kernel_launch(void* const* d_in, const int* in_sizes, int n_in, void* d_out, int out_size, void* d_ws, size_t ws_size,
                              hipStream_t stream) {
  mega_kernel<<<256, 512, 0, stream>>>((const u16*)d_in[0], (const u16*)d_in[1], (const u16*)d_in[2], (const u16*)d_in[4], (const u16*)d_in[5],
                                       (const u16*)d_in[6], (const u16*)d_in[7], (char*)d_ws, (u16*)d_out);
}
```

```cpp
#include <hip/hip_runtime.h>
#include <hip/hip_bf16.h>
#include <stdint.h>

typedef unsigned short u16;
typedef unsigned long long u64;
typedef short bf16x8 __attribute__((ext_vector_type(8)));
typedef short s16x4 __attribute__((ext_vector_type(4)));
typedef float f32x16 __attribute__((ext_vector_type(16)));
typedef float f32x4 __attribute__((ext_vector_type(4)));
typedef float f32x2 __attribute__((ext_vector_type(2)));
typedef unsigned u32x4 __attribute__((ext_vector_type(4)));
typedef unsigned u32x2 __attribute__((ext_vector_type(2)));
typedef __bf16 bf2 __attribute__((ext_vector_type(2)));
#define LDS_AS __attribute__((address_space(3)))
#define GAS __attribute__((address_space(1)))
typedef LDS_AS const char* lds_cptr;
typedef GAS u64 gu64;
#define RLX_AGENT __ATOMIC_RELAXED, __HIP_MEMORY_SCOPE_AGENT

#define TCHS 3
constexpr float EPS = 1e-5f;
constexpr float QSCALE = 0.22360679774997896f * 1.4426950408889634f;

constexpr unsigned WS_SYNC = 0;
constexpr unsigned WS_F0 = 4096, WS_F1 = WS_F0 + 32768, WS_F1B = WS_F1 + 32768, WS_F2 = WS_F1B + 32768;
constexpr unsigned WS_Q = WS_F2 + 32768;
constexpr unsigned WS_KV = WS_Q + 32 * 64 * 1536;
constexpr unsigned WS_O = WS_KV + 32 * 196608;
constexpr unsigned WS_H = WS_O + 64 * 40 * 1024;
constexpr unsigned WS_XN = WS_H + 64 * 96 * 1024;
constexpr unsigned WS_RS = WS_XN;
constexpr unsigned WS_WT = WS_RS + 32768;
constexpr unsigned WS_END = WS_WT + 8 * 68 * 5 * 1024;

#define MFMA32(a, b, c) __builtin_amdgcn_mfma_f32_32x32x16_bf16(a, b, c, 0, 0, 0)
#define MFMA16(a, b, c) __builtin_amdgcn_mfma_f32_16x16x32_bf16(a, b, c, 0, 0, 0)
#define SB() __builtin_amdgcn_sched_barrier(0)
#define EXP2(v) __builtin_amdgcn_exp2f(v)
#define PK4(a, b, c, d) __builtin_bit_cast(bf16x8, ((u32x4){a, b, c, d}))
#define ST16(v, off) __builtin_amdgcn_raw_buffer_store_b128(v, wsr, off, 0, 16)
#define ST8(v, off) __builtin_amdgcn_raw_buffer_store_b64(v, wsr, off, 0, 16)
#define LD16(off, soff) __builtin_bit_cast(bf16x8, __builtin_amdgcn_raw_buffer_load_b128(wsr, off, soff, 16))

#define STAMP(i)

__device__ __forceinline__ unsigned cvtpk(float lo, float hi) {
  return __builtin_bit_cast(unsigned, __builtin_convertvector((f32x2){lo, hi}, bf2));
}
__device__ __forceinline__ float bflo(unsigned u) { return __uint_as_float(u << 16); }
__device__ __forceinline__ float bfhi(unsigned u) { return __uint_as_float(u & 0xffff0000u); }
__device__ __forceinline__ void glds4(unsigned voff, const void* sbase, unsigned ldsb) {
  unsigned sv;
  asm volatile("s_mov_b32 %0, m0\n\ts_mov_b32 m0, %3\n\ts_nop 0\n\tglobal_load_lds_dword %1, %2\n\ts_mov_b32 m0, %0"
               : "=&s"(sv) : "v"(voff), "s"(sbase), "s"(ldsb) : "memory");
}
__device__ __forceinline__ void glds4s(unsigned voff, const void* sbase, unsigned ldsb) {
  unsigned sv;
  asm volatile("s_mov_b32 %0, m0\n\ts_mov_b32 m0, %3\n\ts_nop 0\n\tglobal_load_lds_dword %1, %2 sc1\n\ts_mov_b32 m0, %0"
               : "=&s"(sv) : "v"(voff), "s"(sbase), "s"(ldsb) : "memory");
}
__device__ __forceinline__ void glds16x3(unsigned voff, const void* sbase, unsigned ldsb) {
  unsigned sv;
  asm volatile("s_mov_b32 %0, m0\n\ts_mov_b32 m0, %3\n\ts_nop 0\n\t"
               "global_load_lds_dwordx4 %1, %2 sc1\n\t"
               "global_load_lds_dwordx4 %1, %2 offset:1024 sc1\n\t"
               "global_load_lds_dwordx4 %1, %2 offset:2048 sc1\n\t"
               "s_mov_b32 m0, %0"
               : "=&s"(sv) : "v"(voff), "s"(sbase), "s"(ldsb) : "memory");
}
__device__ __forceinline__ void glds16x4(unsigned voff, const void* sbase, unsigned ldsb) {
  unsigned sv;
  asm volatile("s_mov_b32 %0, m0\n\ts_mov_b32 m0, %3\n\ts_nop 0\n\t"
               "global_load_lds_dwordx4 %1, %2 sc1\n\t"
               "global_load_lds_dwordx4 %1, %2 offset:1024 sc1\n\t"
               "global_load_lds_dwordx4 %1, %2 offset:2048 sc1\n\t"
               "global_load_lds_dwordx4 %1, %2 offset:3072 sc1\n\t"
               "s_mov_b32 m0, %0"
               : "=&s"(sv) : "v"(voff), "s"(sbase), "s"(ldsb) : "memory");
}
template <int N> __device__ __forceinline__ void wait_vm() { asm volatile("s_waitcnt vmcnt(%0)" ::"i"(N) : "memory"); }
template <int N> __device__ __forceinline__ void wait_vm_bar() {
  __builtin_amdgcn_s_waitcnt(0xC07F);
  asm volatile("s_waitcnt vmcnt(%0) lgkmcnt(0)\n\ts_barrier" ::"i"(N) : "memory");
}
__device__ __forceinline__ void bar_lds() { asm volatile("s_waitcnt lgkmcnt(0)\n\ts_barrier" ::: "memory"); }
__device__ __forceinline__ bf16x8 lds_b128(lds_cptr p) { return *(const LDS_AS bf16x8*)p; }
__device__ __forceinline__ s16x4 lds_tr(lds_cptr p) { return __builtin_amdgcn_ds_read_tr16_b64_v4i16((LDS_AS s16x4*)p); }
__device__ __forceinline__ bf16x8 cat8(s16x4 a, s16x4 b) { return __builtin_shufflevector(a, b, 0, 1, 2, 3, 4, 5, 6, 7); }
__device__ __forceinline__ float xhalf_max(float v) {
  auto rr = __builtin_amdgcn_permlane32_swap(__float_as_uint(v), __float_as_uint(v), false, false);
  return fmaxf(__uint_as_float(rr[0]), __uint_as_float(rr[1]));
}
__device__ __forceinline__ float upper_half(float v) {
  auto rr = __builtin_amdgcn_permlane32_swap(__float_as_uint(v), __float_as_uint(v), false, false);
  return __uint_as_float(rr[1]);
}
__device__ __forceinline__ float gelu_tanh(float x) {
  const float c0 = -2.0f * 0.7978845608028654f * 1.4426950408889634f;
  const float c1 = c0 * 0.044715f;
  float t = fmaf(x * x, c1, c0);
  float e = __builtin_amdgcn_exp2f(x * t);
  return x * __builtin_amdgcn_rcpf(1.0f + e);
}
__device__ __forceinline__ unsigned gelu2(f32x2 x) {
  const float c0 = -2.0f * 0.7978845608028654f * 1.4426950408889634f;
  const f32x2 k0 = {c0, c0}, k1 = {c0 * 0.044715f, c0 * 0.044715f}, one = {1.0f, 1.0f};
  const f32x2 z = x * __builtin_elementwise_fma(x * x, k1, k0);
  const f32x2 d = (f32x2){__builtin_amdgcn_exp2f(z[0]), __builtin_amdgcn_exp2f(z[1])} + one;
  const f32x2 g = x * (f32x2){__builtin_amdgcn_rcpf(d[0]), __builtin_amdgcn_rcpf(d[1])};
  return cvtpk(g[0], g[1]);
}
template <int CTRL, int RMASK> __device__ __forceinline__ float dpp_add(float v) {
  return v + __uint_as_float(__builtin_amdgcn_update_dpp(0u, __float_as_uint(v), CTRL, RMASK, 0xf, false));
}
__device__ __forceinline__ float wave_sum(float v) {
  v = dpp_add<0x111, 0xf>(v); v = dpp_add<0x112, 0xf>(v); v = dpp_add<0x114, 0xf>(v); v = dpp_add<0x118, 0xf>(v);
  v = dpp_add<0x142, 0xa>(v); v = dpp_add<0x143, 0xc>(v);
  return __uint_as_float(__builtin_amdgcn_readlane(__float_as_uint(v), 63));
}
__device__ __forceinline__ u64 ld_flag(const char* ws, unsigned off) { return __hip_atomic_load((gu64*)(ws + off), RLX_AGENT); }
__device__ __forceinline__ void st_flag(char* ws, unsigned off, u64 v) { __hip_atomic_store((gu64*)(ws + off), v, RLX_AGENT); }
__device__ __forceinline__ u64 rfl64(u64 v) {
  return ((u64)(unsigned)__builtin_amdgcn_readfirstlane((unsigned)(v >> 32)) << 32) | (unsigned)__builtin_amdgcn_readfirstlane((unsigned)v);
}
constexpr unsigned SPIN_CAP = 1u << 18;
__device__ __forceinline__ void poll2(const char* ws, unsigned off1, unsigned off2, u64 epoch) {
  u64 a, b, m; unsigned c;
  asm volatile("s_mov_b32 %3, %8\n"
               ".Lpl%=:\n\t"
               "global_load_dwordx2 %0, %4, %6 sc1\n\t"
               "global_load_dwordx2 %1, %5, %6 sc1\n\t"
               "s_waitcnt vmcnt(0)\n\t"
               "v_cmp_ne_u64 %2, %7, %0\n\t"
               "v_cmp_ne_u64 vcc, %7, %1\n\t"
               "s_nop 1\n\t"
               "s_or_b64 %2, %2, vcc\n\t"
               "s_cbranch_scc0 .Lpd%=\n\t"
               "s_sleep 3\n\t"
               "s_sub_u32 %3, %3, 1\n\t"
               "s_cbranch_scc0 .Lpl%=\n"
               ".Lpd%=:"
               : "=&v"(a), "=&v"(b), "=&s"(m), "=&s"(c) : "v"(off1), "v"(off2), "s"(ws), "s"(epoch), "i"(SPIN_CAP) : "memory", "vcc", "scc");
}
__device__ __forceinline__ void lds_wait(unsigned la, unsigned want) {
  unsigned v, sv, c;
  asm volatile("s_mov_b32 %2, %5\n"
               ".Lwl%=:\n\t"
               "ds_read_b32 %0, %3\n\t"
               "s_waitcnt lgkmcnt(0)\n\t"
               "v_readfirstlane_b32 %1, %0\n\t"
               "s_nop 1\n\t"
               "s_cmp_ge_u32 %1, %4\n\t"
               "s_cbranch_scc1 .Lwd%=\n\t"
               "s_sleep 2\n\t"
               "s_sub_u32 %2, %2, 1\n\t"
               "s_cbranch_scc0 .Lwl%=\n"
               ".Lwd%=:"
               : "=&v"(v), "=&s"(sv), "=&s"(c) : "v"(la), "s"(want), "i"(SPIN_CAP * 8) : "memory", "scc");
}

constexpr int P1_SLOT = 32768, P1_W = 16384;
constexpr int P1_TOP = 4 * P1_SLOT;
constexpr int P1_B1 = P1_TOP + 24576, P1_CNT = P1_B1 + 256, P1_TCH = P1_CNT + 256, P1_CSA = P1_TCH + 256, P1_RSV = P1_CSA + 2048;
constexpr int LDS_BYTES = 163840;
static_assert(P1_RSV + 2048 <= LDS_BYTES, "lds");
constexpr unsigned OOB = 0x7ffffff0u;

__device__ __forceinline__ void glds16(unsigned voff, const void* sbase, unsigned ldsb) {
  unsigned sv;
  asm volatile("s_mov_b32 %0, m0\n\ts_mov_b32 m0, %3\n\ts_nop 0\n\tglobal_load_lds_dwordx4 %1, %2 sc1\n\ts_mov_b32 m0, %0"
               : "=&s"(sv) : "v"(voff), "s"(sbase), "s"(ldsb) : "memory");
}
__device__ __forceinline__ void glds16p(unsigned voff, const void* sbase, unsigned ldsb) {
  unsigned sv;
  asm volatile("s_mov_b32 %0, m0\n\ts_mov_b32 m0, %3\n\ts_nop 0\n\tglobal_load_lds_dwordx4 %1, %2\n\ts_mov_b32 m0, %0"
               : "=&s"(sv) : "v"(voff), "s"(sbase), "s"(ldsb) : "memory");
}
__device__ __forceinline__ float addf(float a, float b) { float r; asm("v_add_f32_e32 %0, %1, %2" : "=v"(r) : "v"(a), "v"(b)); return r; }
__device__ __forceinline__ float mulf(float a, float b) { float r; asm("v_mul_f32_e32 %0, %1, %2" : "=v"(r) : "v"(a), "v"(b)); return r; }
__device__ __forceinline__ float fmaf1(float a, float b, float c) { float r; asm("v_fma_f32 %0, %1, %2, %3" : "=v"(r) : "v"(a), "v"(b), "v"(c)); return r; }
__device__ __forceinline__ void colsum(f32x4& cs, const bf16x8& sel, const bf16x8& f) { cs = MFMA16(sel, f, cs); SB(); }
__device__ __forceinline__ bf16x8 split_a(float v) {
  const unsigned h = cvtpk(v, 0.f) & 0xffffu, pk = h | (cvtpk(v - bflo(h), 0.f) << 16);
  return PK4(pk, pk, 0u, 0u);
}
__device__ __forceinline__ bf16x8 split_b(float v) {
  const unsigned h = cvtpk(v, v);
  const float l = v - bflo(h);
  return PK4(h, cvtpk(l, l), 0u, 0u);
}
struct P1Dma {
  unsigned vo[4], vm[3];
  const char *base, *base1, *wm, *wm0;
  unsigned kstride;
  unsigned ldsw, lstep, ldsm;
};
template <int S, int J> __device__ __forceinline__ void p1_piece(const P1Dma& d) {
  if (S < 10) glds16p(d.vo[J], ((S & 1) ? d.base1 : d.base) + (S & ~1) * d.kstride, d.ldsw + (S & 3) * P1_SLOT + J * d.lstep);
  else if (J < 3) glds16p(d.vm[J < 3 ? J : 0], d.wm + 2 * (S - 10) * (64 * 3072), d.ldsm + (S == 14 ? P1_TOP : (S & 3) * P1_SLOT) + J * 4096);
}
template <int S> __device__ __forceinline__ void p1_issue(const P1Dma& d) { p1_piece<S, 0>(d); p1_piece<S, 1>(d); p1_piece<S, 2>(d); p1_piece<S, 3>(d); }

struct FragA { bf16x8 x0[2], x1[4], w[4]; };
struct PtrA { lds_cptr x0[4], x1[4], w[4]; LDS_AS float* csw; const char* ws; unsigned g0, g1, gs, tv, tl; int tdo; };
struct StatA { f32x4 cs; bf16x8 sel; u32x4 gk, go, xa, xb; u64 ev, epoch; unsigned tag, stc, str_; };
template <int S, int J> __device__ __forceinline__ void rd_a(FragA& f, bf16x8 (&xk)[20], const PtrA& p) {
  constexpr int so = (S & 3) * P1_SLOT;
  if (J < 2) xk[2 * S + (J & 1)] = lds_b128(p.x0[J] + so); else f.x0[J & 1] = lds_b128(p.x0[J] + so);
  f.w[J] = cat8(lds_tr(p.w[J] + so), lds_tr(p.w[J] + so + 1024));
  f.x1[J] = lds_b128(p.x1[J] + so);
}
template <bool VT> __device__ __forceinline__ void mm_a(f32x16& acc, const bf16x8& xf, const bf16x8& wf) {
  SB(); acc = VT ? MFMA32(xf, wf, acc) : MFMA32(wf, xf, acc); SB();
}
template <bool VT, int S>
__device__ __forceinline__ void p1_stage_a(f32x16 (&acc)[2], FragA& fn, const FragA& fc, bf16x8 (&xk)[20], const PtrA& p, const P1Dma& d, StatA& st) {
  wait_vm_bar<(S == 0 ? 4 : S == 1 ? 8 : S == 2 ? 9 : S < 5 ? 12 : S == 5 ? 11 : S == 6 ? 8 : S == 7 ? 9 : S == 8 ? 8 : 6)>();
  if (S == 9) asm volatile("" : "+v"(st.gk), "+v"(st.go));
#define CS(E, J) if (S < 9 && E == 1) colsum(st.cs, st.sel, fc.w[J])
  mm_a<VT>(acc[0], xk[2 * S], fc.w[0]);     if (S < 9) rd_a<(S + 1) % 10, 0>(fn, xk, p); CS(0, 0); if (S == 0) { p1_piece<3, 0>(d); p1_piece<3, 1>(d); }
  mm_a<VT>(acc[1], fc.x1[0], fc.w[0]);      if (S < 9) rd_a<(S + 1) % 10, 2>(fn, xk, p); CS(1, 0); if (S == 0) { p1_piece<3, 2>(d); p1_piece<3, 3>(d); }
  mm_a<VT>(acc[0], xk[2 * S + 1], fc.w[1]); p1_piece<S + 4, 0>(d); CS(2, 0);
  mm_a<VT>(acc[1], fc.x1[1], fc.w[1]);      if (S < 9) rd_a<(S + 1) % 10, 1>(fn, xk, p); CS(3, 0);
  mm_a<VT>(acc[0], fc.x0[0], fc.w[2]);      p1_piece<S + 4, 1>(d); CS(0, 2);
  mm_a<VT>(acc[1], fc.x1[2], fc.w[2]);      if (S < 9) rd_a<(S + 1) % 10, 3>(fn, xk, p); CS(1, 2);
  mm_a<VT>(acc[0], fc.x0[1], fc.w[3]);      p1_piece<S + 4, 2>(d); CS(2, 2);
  mm_a<VT>(acc[1], fc.x1[3], fc.w[3]);      p1_piece<S + 4, 3>(d); CS(3, 2);
#undef CS
  if (S == 1) {
    asm volatile("" : "+v"(st.ev), "+v"(st.xa), "+v"(st.xb));
    st.epoch = rfl64(st.ev) + 1;
    st.tag = (unsigned)st.epoch ^ (unsigned)(st.epoch >> 32);
    const float c = __uint_as_float(__builtin_amdgcn_readfirstlane(st.xa[0]) << 16);
    float s1 = 0.f, s2 = 0.f, t1 = 0.f, t2 = 0.f;
#pragma unroll
    for (int e = 0; e < 4; ++e) {
      const float a0 = bflo(st.xa[e]) - c, a1 = bfhi(st.xa[e]) - c, b0 = bflo(st.xb[e]) - c, b1v = bfhi(st.xb[e]) - c;
      s1 += a0 + a1; s2 = fmaf(a0, a0, fmaf(a1, a1, s2)); t1 += b0 + b1v; t2 = fmaf(b0, b0, fmaf(b1v, b1v, t2));
    }
    if ((threadIdx.x & 63) < 16) { s1 += t1; s2 += t2; }
    const float m = wave_sum(s1) * (1.0f / 640.0f);
    const float var = fmaxf(wave_sum(s2) * (1.0f / 640.0f) - m * m, 0.0f);
    const float r1 = __builtin_amdgcn_rsqf(var + EPS);
    const float r2 = __builtin_amdgcn_rsqf(var * r1 * r1 + EPS);
    const auto wsr = __builtin_amdgcn_make_buffer_rsrc((char*)p.ws, 0, WS_END, 0x00020000);
    ST16(((u32x4){__float_as_uint(m + c), __float_as_uint(r1), __float_as_uint(r1 * r2), st.tag}), p.gs);
    SB();
  }
  if (S == 2) p1_issue<14>(d);
  if (S == TCHS && p.tdo) glds4(p.tv, d.wm0, p.tl);
  if (S == 6)
    asm volatile("global_load_dwordx4 %0, %2, %4 sc1\n\tglobal_load_dwordx4 %1, %3, %4 sc1" : "=&v"(st.gk), "=&v"(st.go) : "v"(p.g0), "v"(p.g1), "s"(p.ws) : "memory");
  if (S == 8) { colsum(st.cs, st.sel, fn.w[0]); colsum(st.cs, st.sel, fn.w[2]); p.csw[0] = st.cs[0]; p.csw[32] = st.cs[1]; }
}
template <bool VT>
__device__ __forceinline__ void p1_pass_a(f32x16 (&acc)[2], bf16x8 (&xk)[20], const PtrA& p, const P1Dma& d, StatA& st) {
  FragA f0, f1;
  wait_vm_bar<4>();
  p1_issue<2>(d);
  rd_a<0, 0>(f0, xk, p); rd_a<0, 2>(f0, xk, p); rd_a<0, 1>(f0, xk, p); rd_a<0, 3>(f0, xk, p);
  p1_stage_a<VT, 0>(acc, f1, f0, xk, p, d, st); p1_stage_a<VT, 1>(acc, f0, f1, xk, p, d, st);
  p1_stage_a<VT, 2>(acc, f1, f0, xk, p, d, st); p1_stage_a<VT, 3>(acc, f0, f1, xk, p, d, st);
  p1_stage_a<VT, 4>(acc, f1, f0, xk, p, d, st); p1_stage_a<VT, 5>(acc, f0, f1, xk, p, d, st);
  p1_stage_a<VT, 6>(acc, f1, f0, xk, p, d, st); p1_stage_a<VT, 7>(acc, f0, f1, xk, p, d, st);
  p1_stage_a<VT, 8>(acc, f1, f0, xk, p, d, st); p1_stage_a<VT, 9>(acc, f0, f1, xk, p, d, st);
}
struct FragB { bf16x8 w[6]; };
constexpr int cbK(int C, int Q) { return C < 4 ? (C + 8) % 10 : 2 + 3 * ((C - 4) & 1) + Q / 2; }
constexpr int cbJ(int C, int Q) { return C < 4 ? Q / 3 : Q & 1; }
constexpr int cbT(int C, int Q) { return C < 4 ? Q % 3 : (C - 4) / 2; }
template <int C, int Q> __device__ __forceinline__ void rd_b(FragB& f, const lds_cptr (&wpb)[2][3]) {
  constexpr int K = cbK(C, Q), J = cbJ(C, Q);
  constexpr int slot = K >= 8 ? P1_TOP : ((10 + K / 2) & 3) * P1_SLOT;
  constexpr int off = slot + (K & 1) * 12288 + cbT(C, Q) * 64;
  f.w[Q] = cat8(lds_tr(wpb[J][off >> 16] + (off & 0xffff)), lds_tr(wpb[J][off >> 16] + (off & 0xffff) + 768));
}
template <int C, int Q, class HK>
__device__ __forceinline__ void mm_b(f32x16 (&acc)[3], f32x4 (&cs)[3], const bf16x8& sel, FragB& fn, const FragB& fc, const bf16x8 (&xk)[20],
                                     const lds_cptr (&wpb)[2][3], HK&& hook) {
  mm_a<false>(acc[cbT(C, Q)], xk[2 * cbK(C, Q) + cbJ(C, Q)], fc.w[Q]);
  if (C < 9) rd_b<(C + 1) % 10, Q>(fn, wpb);
  hook(Q);
  SB();
  if (cbJ(C, Q) == 0) colsum(cs[cbT(C, Q)], sel, fc.w[Q]);
}
template <int C, class HK>
__device__ __forceinline__ void p1_chunk_b(f32x16 (&acc)[3], f32x4 (&cs)[3], const bf16x8& sel, FragB& fn, const FragB& fc, const bf16x8 (&xk)[20],
                                           const lds_cptr (&wpb)[2][3], HK&& hook) {
  mm_b<C, 0>(acc, cs, sel, fn, fc, xk, wpb, hook); mm_b<C, 1>(acc, cs, sel, fn, fc, xk, wpb, hook); mm_b<C, 2>(acc, cs, sel, fn, fc, xk, wpb, hook);
  mm_b<C, 3>(acc, cs, sel, fn, fc, xk, wpb, hook); mm_b<C, 4>(acc, cs, sel, fn, fc, xk, wpb, hook); mm_b<C, 5>(acc, cs, sel, fn, fc, xk, wpb, hook);
}
template <int N> __device__ __forceinline__ void vm_bar() { asm volatile("s_waitcnt vmcnt(%0)\n\ts_barrier" ::"i"(N) : "memory"); }

constexpr int P2_STG = 24576;
constexpr int P3_WO = 81920;
constexpr int P3_C2 = P1_CNT + 16;
template <int T, class HK>
__device__ __forceinline__ void attn_step(HK&& hook, f32x16& Sc, f32x16& Sn, f32x16& o, const bf16x8& q0, const bf16x8& q1, bf16x8& k0, bf16x8& k1,
                                          bf16x8& v0, bf16x8& v1, unsigned (&p)[8], float (&e)[4], lds_cptr kc, lds_cptr kn, lds_cptr vc,
                                          lds_cptr vn, int k1off) {
  const f32x16 zero = {};
  const lds_cptr k2p = (T + 2 < 8) ? kc + (T + 2) * 3072 : kn + (T + 2 - 8) * 3072;
  const lds_cptr v1p = (T + 1 < 8) ? vc + (T + 1) * 3072 : vn;
  float a0, a1, a2, a3;
  Sn = MFMA32(k0, q0, zero);
  k0 = lds_b128(k2p);
  SB();
  p[6] = cvtpk(e[0], e[1]); p[7] = cvtpk(e[2], e[3]);
  SB();
  a0 = EXP2(Sc[0]); a1 = EXP2(Sc[1]);
  SB(); hook(4 * T); SB();
  a2 = EXP2(Sc[2]); a3 = EXP2(Sc[3]);
  SB();
  o = MFMA32(v1, PK4(p[4], p[5], p[6], p[7]), o);
  v1 = lds_b128(vc + T * 3072 + 768);
  SB();
  p[0] = cvtpk(a0, a1); p[1] = cvtpk(a2, a3);
  SB();
  a0 = EXP2(Sc[4]); a1 = EXP2(Sc[5]);
  SB(); hook(4 * T + 1); SB();
  a2 = EXP2(Sc[6]); a3 = EXP2(Sc[7]);
  SB();
  Sn = MFMA32(k1, q1, Sn);
  k1 = lds_b128(k2p + k1off);
  SB();
  p[2] = cvtpk(a0, a1); p[3] = cvtpk(a2, a3);
  SB();
  a0 = EXP2(Sc[8]); a1 = EXP2(Sc[9]);
  SB(); hook(4 * T + 2); SB();
  a2 = EXP2(Sc[10]); a3 = EXP2(Sc[11]);
  SB();
  o = MFMA32(v0, PK4(p[0], p[1], p[2], p[3]), o);
  v0 = lds_b128(v1p);
  SB();
  p[4] = cvtpk(a0, a1); p[5] = cvtpk(a2, a3);
  SB();
  e[0] = EXP2(Sc[12]); e[1] = EXP2(Sc[13]);
  SB(); hook(4 * T + 3); SB();
  e[2] = EXP2(Sc[14]); e[3] = EXP2(Sc[15]);
  SB();
}

__device__ __forceinline__ void setprio_rt(int p) {
  if (p == 0) __builtin_amdgcn_s_setprio(0); else if (p == 1) __builtin_amdgcn_s_setprio(1); else if (p == 2) __builtin_amdgcn_s_setprio(2); else __builtin_amdgcn_s_setprio(3);
}
__device__ __forceinline__ int v_colblock(int tile) { return tile < 48 ? tile >> 2 : 12 + (tile - 48) / 3; }

__global__ __launch_bounds__(512, 2) void mega_kernel(const u16* __restrict__ x, const u16* __restrict__ wqkv, const u16* __restrict__ wo,
                                                       const u16* __restrict__ w1, const u16* __restrict__ b1, const u16* __restrict__ w2,
                                                       const u16* __restrict__ b2, char* ws, u16* __restrict__ out) {
  __shared__ __attribute__((aligned(16))) char lds[LDS_BYTES];
  const unsigned lds0 = (unsigned)(uintptr_t)(LDS_AS char*)lds;
  const int b = blockIdx.x, xcd = b & 7, i = b >> 3;
  const int tid = threadIdx.x, lane = tid & 63, w = __builtin_amdgcn_readfirstlane(tid >> 6);
  const int r = lane & 31, hh = lane >> 5, tt = w & 3;
  const auto wsr = __builtin_amdgcn_make_buffer_rsrc(ws, 0, WS_END, 0x00020000);
  STAMP(0);
  asm volatile("" :: "s"(x), "s"(wqkv), "s"(wo), "s"(w1), "s"(b1), "s"(w2), "s"(b2), "s"(ws), "s"(out));
  const int head = xcd * 4 + (i & 3), qb = i >> 2;
  const int p = (xcd & 1) * 8 + (i & 7), cb = (xcd >> 1) * 4 + (i >> 3);
  const int id1 = p * 16 + cb;
  u64 epoch_v; u32x4 xa, xb;
  {
    const unsigned xo = (unsigned)((p * 128 + cb * 8 + w) * 1280);
    const unsigned eo = ((WS_F1 + id1 * 128) & -(unsigned)(tid == 1)) | ((WS_F1B + b * 128) & -(unsigned)(tid == 2)) |
                        ((WS_F2 + (qb * 32 + head) * 128) & -(unsigned)(tid == 3)) | ((WS_RS + (p * 128 + cb * 8) * 16) & -(unsigned)(tid == 4));
    static_assert(WS_SYNC == 0, "eo = 0 selects the epoch word");
    asm volatile("global_load_dwordx2 %0, %3, %6 sc1\n\tglobal_load_dwordx4 %1, %4, %7\n\tglobal_load_dwordx4 %2, %5, %7"
                 : "=&v"(epoch_v), "=&v"(xa), "=&v"(xb) : "v"(eo), "v"(xo + lane * 16), "v"(xo + 1024 + (lane & 15) * 16), "s"(ws), "s"(x) : "memory");
  }
  {
    u64 pc; unsigned len;
    asm volatile("s_getpc_b64 %0\n.Lmk_start:\n\ts_mov_b32 %1, .Lmk_end-.Lmk_start" : "=s"(pc), "=s"(len));
    if (w == 6) glds4(min((unsigned)(i * 9 + min(lane, 8)) * 128u, len), (const char*)pc, lds0 + P1_TCH);
  }
  if (tid < 8) ((LDS_AS unsigned*)(LDS_AS char*)&lds[P1_CNT])[tid] = 0u;
  u64 epoch;
  const char* kvg = ws + WS_KV + head * 196608;
  const unsigned vdma = (unsigned)(w * 3072 + lane * 16);
  const unsigned qo = WS_Q + (head * 64 + qb * 8 + w) * 1536;
  bf16x8 q0; u32x4 q1u;

  {
    const int qt0 = cb < 12 ? cb * 4 : 48 + (cb - 12) * 3;
    const int nq = cb < 12 ? 4 : 3;
    const int wh = w >> 2, nt = w >> 1, tp = w & 1;
    const int tk = 2 * tp + (nt & 1), to = tk ^ 1;
    P1Dma d;
    if (w >= 4) {
      const int qr = 4 * tt + (lane >> 4);
#pragma unroll
      for (int q = 0; q < 4; ++q)
        d.vo[q] = (unsigned)((qr + 16 * q) * 3840 + (qt0 + min((((lane >> 2) & 3) ^ (qr & 3)), nq - 1)) * 64 + (lane & 3) * 16);
      d.base = (const char*)wqkv; d.kstride = 64 * 3840; d.ldsw = lds0 + P1_W + tt * 1024; d.lstep = 4096;
    } else {
#pragma unroll
      for (int q = 0; q < 4; ++q) {
        const int row = 32 * tt + 8 * q + (lane >> 3);
        d.vo[q] = (unsigned)((p * 128 + row) * 1280 + (((lane & 7) ^ ((row >> 1) & 7)) << 4));
      }
      d.base = (const char*)x; d.kstride = 128; d.ldsw = lds0 + tt * 4096; d.lstep = 1024;
    }
    const int kp = ((i >> 3) ^ i) & 1;
    d.base1 = d.base + (kp ^ 1) * d.kstride; d.base += kp * d.kstride;
    if (w == 7) glds4((unsigned)(cb * 192 + min(lane, 47) * 4), b1, lds0 + P1_B1);
    p1_issue<0>(d); p1_issue<1>(d);
    STAMP(25);
    d.wm = (const char*)w1 + (wh ^ kp) * (64 * 3072); d.ldsm = lds0 + wh * 12288 + tt * 1024;
#pragma unroll
    for (int q = 0; q < 3; ++q) { const int u = q * 256 + tt * 64 + lane; d.vm[q] = (unsigned)((u / 12) * 3072 + cb * 192 + (u % 12) * 16); }
    PtrA pa;
    d.wm0 = (const char*)w1;
    pa.tdo = w >= 4; pa.tl = lds0 + P1_TCH;
    pa.tv = (unsigned)(((i & 7) * 64 + tt * 16 + ((lane >> 1) & 15)) * 3072 + cb * 192 + (lane & 1) * 188);
    pa.gs = lane == 0 ? WS_RS + (p * 128 + cb * 8 + w) * 16 : OOB;
    {
      const int q4 = (lane & 15) >> 2;
      const lds_cptr wb = (lds_cptr)lds + P1_W + (8 * hh + q4) * 256 + (nt ^ q4) * 64 + ((lane >> 4) & 1) * 32 + (lane & 3) * 8;
#pragma unroll
      for (int j = 0; j < 4; ++j) {
        const int ks = 2 * ((j >> 1) ^ wh) + ((j & 1) ^ tp);
        const int cs = ((2 * ks + hh) ^ ((r >> 1) & 7)) << 4;
        pa.x0[j] = (lds_cptr)lds + (tk * 32 + r) * 128 + cs;
        pa.x1[j] = (lds_cptr)lds + (to * 32 + r) * 128 + cs;
        pa.w[j] = wb + ks * 4096;
      }
      pa.csw = (LDS_AS float*)(LDS_AS char*)&lds[P1_CSA] + (lane < 16 ? nt * 64 + lane * 2 + tp : 256 + lane);
      pa.ws = ws; pa.g0 = WS_RS + (p * 128 + tk * 32 + r) * 16; pa.g1 = WS_RS + (p * 128 + to * 32 + r) * 16;
    }
    lds_cptr wpb[2][3];
#pragma unroll
    for (int j = 0; j < 2; ++j) {
      wpb[j][0] = (lds_cptr)lds + (32 * wh + 16 * (j ^ tp) + 8 * hh + ((lane & 15) >> 2)) * 192 + (((lane >> 4) & 1) ^ wh) * 32 + (lane & 3) * 8;
      wpb[j][1] = wpb[j][0] + 65536; wpb[j][2] = wpb[j][0] + 131072;
    }
    bf16x8 xk[20];
    FragB g0, g1;
    StatA st;
    st.cs = (f32x4){};
    { const unsigned sv = (lane & 31) == 0 || (lane & 31) == 17 ? 0x3f803f80u : 0u; st.sel = PK4(sv, sv, sv, sv); }
    st.xa = xa; st.xb = xb; st.ev = epoch_v;
    {
      f32x16 acc[2] = {};
      if (cb >= 10) p1_pass_a<true>(acc, xk, pa, d, st);
      else p1_pass_a<false>(acc, xk, pa, d, st);
      STAMP(3);
      epoch = st.epoch;
      const unsigned tag = st.tag;
      const f32x2 csp = ((LDS_AS f32x2*)(LDS_AS char*)&lds[P1_CSA])[nt * 32 + r];
      rd_b<0, 0>(g0, wpb); rd_b<0, 1>(g0, wpb); rd_b<0, 2>(g0, wpb); rd_b<0, 3>(g0, wpb); rd_b<0, 4>(g0, wpb); rd_b<0, 5>(g0, wpb);
      SB();
      if (__builtin_expect(!__all(st.gk[3] == tag && st.go[3] == tag), 0)) {
#pragma nounroll
        for (unsigned sp = 0; sp < SPIN_CAP; ++sp) {
          st.gk = __builtin_bit_cast(u32x4, LD16(pa.g0, 0)); st.go = __builtin_bit_cast(u32x4, LD16(pa.g1, 0));
          if (__all(st.gk[3] == tag && st.go[3] == tag)) break;
          __builtin_amdgcn_s_sleep(3);
          asm volatile("" ::: "memory");
        }
      }
      const bf16x8 csf = split_a(hh ? 0.0f : csp[0] + csp[1]);
      const bf16x8 mk = split_b(-__uint_as_float(st.gk[0])), mo = split_b(-__uint_as_float(st.go[0]));
      if (cb >= 10) {
        acc[0] = MFMA32(mk, csf, acc[0]); acc[1] = MFMA32(mo, csf, acc[1]);
        LDS_AS float* rsv = (LDS_AS float*)(LDS_AS char*)&lds[P1_RSV] + w * 64;
        rsv[lane] = __uint_as_float(hh ? st.go[1] : st.gk[1]);
        const int n = (qt0 + nt) * 32 + r - 1280;
        const int hd = n / 20, dd = n - hd * 20;
        const unsigned ov = dd == 16 ? 0x3f803f80u : 0u;
#pragma unroll
        for (int t = 0; t < 2; ++t) {
          unsigned pk[8];
#pragma unroll
          for (int g = 0; g < 4; ++g) {
            const f32x4 rr = *(const LDS_AS f32x4*)(rsv + t * 32 + 8 * g + 4 * hh);
            pk[2 * g] = cvtpk(acc[t][4 * g] * rr[0], acc[t][4 * g + 1] * rr[1]); pk[2 * g + 1] = cvtpk(acc[t][4 * g + 2] * rr[2], acc[t][4 * g + 3] * rr[3]);
          }
          const unsigned vp = nt < nq ? WS_KV + hd * 196608 + (p * 4 + (t ? to : tk)) * 3072 + 1536 + (hh * 24 + dd) * 16 : OOB;
          ST16(((u32x4){pk[0], pk[1], pk[2], pk[3]}), vp);
          ST16(((u32x4){pk[4], pk[5], pk[6], pk[7]}), vp + 768);
          const unsigned vq = dd >= 16 ? vp + 64 : OOB;
          ST16(((u32x4){ov, ov, ov, ov}), vq);
          ST16(((u32x4){ov, ov, ov, ov}), vq + 768);
        }
      } else {
        acc[0] = MFMA32(csf, mk, acc[0]); acc[1] = MFMA32(csf, mo, acc[1]);
        const bool isq = cb < 5;
        const float qs = isq ? QSCALE : 1.0f;
        const float rs2[2] = {qs * __uint_as_float(st.gk[1]), qs * __uint_as_float(st.go[1])};
        const unsigned ts = isq ? 1536u : 3072u;
        const unsigned base = (isq ? WS_Q : WS_KV) + p * 4 * ts + r * 16;
        const unsigned hs = isq ? 64u * 1536u : 196608u;
        const unsigned cst = isq ? 0u : 0x3f80u;
#pragma unroll
        for (int g = 0; g < 4; g += 2) {
          const unsigned os = (qt0 + nt) * 4 + g + hh - (isq ? 0 : 80);
          const unsigned hp = os / 5u, oi = os - hp * 5u;
          const bool sp = oi == 2;
          const unsigned a0 = base + (2 * hp + (oi > 2)) * hs + (oi > 2 ? oi - 3 : oi) * 512;
#pragma unroll
          for (int t = 0; t < 2; ++t) {
            const float rs = rs2[t];
            const auto s0 = __builtin_amdgcn_permlane32_swap(cvtpk(acc[t][4 * g] * rs, acc[t][4 * g + 1] * rs),
                                                             cvtpk(acc[t][4 * g + 4] * rs, acc[t][4 * g + 5] * rs), false, false);
            const auto s1 = __builtin_amdgcn_permlane32_swap(cvtpk(acc[t][4 * g + 2] * rs, acc[t][4 * g + 3] * rs),
                                                             cvtpk(acc[t][4 * g + 6] * rs, acc[t][4 * g + 7] * rs), false, false);
            const unsigned a1 = a0 + (t ? to : tk) * ts;
            ST16(((u32x4){s0[0], s1[0], sp ? cst : s0[1], sp ? 0u : s1[1]}), a1);
            ST16(((u32x4){s0[1], s1[1], cst, 0u}), sp ? a1 + hs : OOB);
          }
        }
      }
      SB();
    }
    STAMP(16);
    const float mean_k = __uint_as_float(st.gk[0]), rs = __uint_as_float(st.gk[2]);
    f32x16 acc[3] = {};
    f32x4 cs[3] = {};
    auto nohk = [](int) {};
    p1_chunk_b<0>(acc, cs, st.sel, g1, g0, xk, wpb, nohk); p1_chunk_b<1>(acc, cs, st.sel, g0, g1, xk, wpb, nohk);
    p1_chunk_b<2>(acc, cs, st.sel, g1, g0, xk, wpb, nohk);
    vm_bar<8>();
    STAMP(17);
    const int bk = w * 256 + b;
    const int rto = w == 0 ? 24576 : w == 1 ? 57344 : 65536;
    if (bk < 544) {
      const int kk = bk % 68, c8 = bk / 68;
      const int r0 = kk < 48 ? 32 * kk : kk < 64 ? 40 * (kk - 48) : 160 * (kk - 64) + 16;
      const int ra = kk < 48 ? 16 : kk < 64 ? 20 : 80, rbb = kk < 64 ? 8 : 20, rc = kk < 64 ? 4 : 40;
#pragma unroll
      for (int q = 0; q < 5; ++q) {
        const int u = q * 64 + lane, rho = u / 10;
        const int row = r0 + ra * (rho >> 4) + rc * ((rho >> 3) & 1) + rbb * ((rho >> 2) & 1) + (rho & 3);
        glds16p((unsigned)(row * 1280 + c8 * 160 + (u % 10) * 16), kk < 48 ? w2 : wo, lds0 + rto + q * 1024);
      }
    }
    const unsigned hp = WS_H + ((p * 4 + tk) * 96 + cb * 6 + wh) * 1024 + lane * 16;
    const bf16x8 mk = split_b(-mean_k);
    auto xbase = [&](int t) { return (LDS_AS char*)&lds[t == 1 ? 73728 : P1_TOP]; };
    auto put = [&](int t) {
      LDS_AS f32x4* xch = (LDS_AS f32x4*)xbase(t);
      LDS_AS float* cst = (LDS_AS float*)(xbase(t) + 16384) + (lane < 16 ? (lane ^ (wh << 4)) * 4 + wh * 2 + tp : 128 + lane);
      cst[0] = cs[t][0]; cst[lane < 16 ? (wh ? -64 : 64) : 0] = cs[t][1];
#pragma unroll
      for (int u = 0; u < 2; ++u)
        xch[(w * 2 + u) * 64 + lane] = (f32x4){acc[t][8 + 4 * u], acc[t][9 + 4 * u], acc[t][10 + 4 * u], acc[t][11 + 4 * u]};
    };
    f32x4 px[2], cc; u32x2 bb[2];
    float gx[2], gz[2]; unsigned pk[4];
    auto get = [&](int t) {
      cc = *(const LDS_AS f32x4*)(xbase(t) + 16384 + (r ^ (wh << 4)) * 16);
#pragma unroll
      for (int u = 0; u < 2; ++u) {
        px[u] = ((const LDS_AS f32x4*)xbase(t))[((w ^ 4) * 2 + u) * 64 + lane];
        bb[u] = *(const u32x2*)(lds + P1_B1 + (t * 32 + 16 * wh + 8 * u + 4 * hh) * 2);
      }
    };
    auto corr = [&](int t) { acc[t] = MFMA32(split_a(__uint_as_float(__float_as_uint(addf(addf(cc[0], cc[1]), addf(cc[2], cc[3]))) & (unsigned)(hh - 1))), mk, acc[t]); };
    const float gc0 = -2.0f * 0.7978845608028654f * 1.4426950408889634f, gc1 = gc0 * 0.044715f;
    auto gelu_a = [&](int t, int q) {
      if (q == 0) { asm volatile("s_nop 7"); SB(); }
#pragma unroll
      for (int e = 0; e < 2; ++e) {
        const unsigned bw = bb[q >> 1][q & 1];
        gx[e] = fmaf1(addf(acc[t][2 * q + e], px[q >> 1][(2 * q + e) & 3]), rs, e ? bfhi(bw) : bflo(bw));
        gz[e] = EXP2(mulf(gx[e], fmaf1(mulf(gx[e], gx[e]), gc1, gc0)));
      }
      asm volatile("" : "+v"(gx[0]), "+v"(gx[1]), "+v"(gz[0]), "+v"(gz[1]));
    };
    auto gelu_b = [&](int t, int q) {
      asm("v_add_f32_e32 %0, 1.0, %0\n\tv_add_f32_e32 %1, 1.0, %1\n\tv_rcp_f32_e32 %0, %0\n\tv_rcp_f32_e32 %1, %1\n\t"
          "v_mul_f32_e32 %0, %2, %0\n\tv_mul_f32_e32 %1, %3, %1" : "+v"(gz[0]), "+v"(gz[1]) : "v"(gx[0]), "v"(gx[1]));
      pk[q] = cvtpk(gz[0], gz[1]);
      asm volatile("" : "+v"(pk[q]));
      if (q == 3) ST16(((u32x4){pk[0], pk[1], pk[2], pk[3]}), hp + t * 2048);
    };
    auto epi = [&](int t, int n) {
      if (n == 1) corr(t);
      if (n >= 4) { if (n & 1) gelu_b(t, (n - 4) >> 1); else gelu_a(t, (n - 4) >> 1); }
    };
    p1_chunk_b<3>(acc, cs, st.sel, g0, g1, xk, wpb, nohk);
    p1_chunk_b<4>(acc, cs, st.sel, g1, g0, xk, wpb, nohk); p1_chunk_b<5>(acc, cs, st.sel, g0, g1, xk, wpb, nohk);
    put(0);
    STAMP(18);
    if (bk < 544) wait_vm_bar<5>(); else wait_vm_bar<0>();
    STAMP(19);
    if (tid == 0) st_flag(ws, WS_F1 + id1 * 128, epoch);
    get(0);
    p1_chunk_b<6>(acc, cs, st.sel, g1, g0, xk, wpb, [&](int n) { epi(0, n); }); p1_chunk_b<7>(acc, cs, st.sel, g0, g1, xk, wpb, [&](int n) { epi(0, n + 6); });
    put(1);
    bar_lds();
    get(1);
    if (bk < 544) {
      wait_vm<1>();
      const lds_cptr rp = (lds_cptr)lds + rto + (8 * (lane >> 4) + ((lane & 15) >> 2)) * 160 + (lane & 3) * 8;
#pragma unroll
      for (int t = 0; t < 5; ++t)
        ST16(__builtin_bit_cast(u32x4, cat8(lds_tr(rp + t * 32), lds_tr(rp + t * 32 + 640))), WS_WT + (bk * 5 + t) * 1024 + lane * 16);
    }
    p1_chunk_b<8>(acc, cs, st.sel, g1, g0, xk, wpb, [&](int n) { epi(1, n); });
    unsigned fo1 = 0, fo2 = 0;
    u64 fa = 0, fb = 0;
    if (w == 3) {
      const int ca = (head * 20) >> 7, cbb = (head * 20 + 19) >> 7;
      const int va = v_colblock(40 + ((head * 20) >> 5)), vb = v_colblock(40 + ((head * 20 + 19) >> 5));
      const int sel = lane >> 4;
      const int c1 = sel == 0 ? 5 + ca : sel == 1 ? 5 + cbb : sel == 2 ? va : vb;
      fo1 = WS_F1 + ((lane & 15) * 16 + c1) * 128;
      fo2 = WS_F1 + ((2 * qb + (lane & 1)) * 16 + ((lane & 2) ? cbb : ca)) * 128;
      fa = ld_flag(ws, fo1); fb = ld_flag(ws, fo2);
    }
    p1_chunk_b<9>(acc, cs, st.sel, g0, g1, xk, wpb, [&](int n) { epi(1, n + 6); });
    STAMP(4);
    put(2);
    if (w == 3) { SB(); if (!__all(fa == epoch && fb == epoch)) poll2(ws, fo1, fo2, epoch); SB(); }
    bar_lds();
    STAMP(20);
    get(2);
    SB();
    glds16x3(vdma, kvg, lds0 + w * 3072);
    q0 = LD16(qo + lane * 16, 0);
    q1u = __builtin_bit_cast(u32x4, LD16(qo + 1024 + r * 16, 0));
    SB();
    glds16x3(vdma, kvg + P2_STG, lds0 + P2_STG + w * 3072);
    SB();
    corr(2);
#pragma unroll
    for (int q = 0; q < 4; ++q) {
      const unsigned bw = bb[q >> 1][q & 1];
      pk[q] = gelu2(((f32x2){acc[2][2 * q], acc[2][2 * q + 1]} + (f32x2){px[q >> 1][(2 * q) & 3], px[q >> 1][(2 * q + 1) & 3]}) * rs + (f32x2){bflo(bw), bfhi(bw)});
    }
    asm volatile("" : "+v"(pk[0]), "+v"(pk[1]), "+v"(pk[2]), "+v"(pk[3]));
    SB();
    asm volatile("" : "+v"(q0), "+v"(q1u));
    glds16x3(vdma, kvg + 2 * P2_STG, lds0 + 2 * P2_STG + w * 3072);
    ST16(((u32x4){pk[0], pk[1], pk[2], pk[3]}), hp + 4096);
    STAMP(21);
  }

  const int rb = xcd * 4 + (i & 3), cq = i >> 2;
  const unsigned ao = (lane >> 5) * 1024 + (((lane >> 4) & 1) * 32 + (lane & 15)) * 16;
  const unsigned wofs = WS_WT + cq * 68 * 5 * 1024 + lane * 16;
  bf16x8 af[4], wf[2][5] = {};
  f32x4 acc[4][5] = {};
  auto lda = [&](int m, int kk) { af[m] = LD16(ao + (m & 1) * 256, WS_H + ((2 * rb + (m >> 1)) * 96 + 2 * kk) * 1024); };
  auto ldw = [&](int s, int kk) {
#pragma unroll
    for (int t = 0; t < 5; ++t) wf[s][t] = LD16(wofs + t * 1024, kk * 5120);
  };
  auto wo_piece = [&](int q) {
    const int n = min(w * 8 + q, 59), j = n / 15;
    glds16(wofs, ws + (48 + 5 * j) * 5120 + (n - 15 * j) * 1024, lds0 + P3_WO + n * 1024);
  };
  {
    if (hh) q1u = (u32x4){0u, 0u, 0u, 0u};
    const int dcl = r < 21 ? r : 21;
    const int k1off = hh ? 1536 + 21 * 16 - lane * 16 : 1024;
    const lds_cptr kb = (lds_cptr)lds + lane * 16;
    const lds_cptr vb = (lds_cptr)lds + 1536 + (hh * 24 + dcl) * 16;
    const f32x16 zero = {};
    wait_vm_bar<4>();
    STAMP(22);
    bf16x8 k0 = lds_b128(kb), k1 = lds_b128(kb + k1off);
    f32x16 sa = MFMA32(k0, q0, zero), sb;
    {
      f32x16 s = MFMA32(k1, __builtin_bit_cast(bf16x8, q1u), sa);
      float m = s[0];
#pragma unroll
      for (int q = 1; q < 16; ++q) m = fmaxf(m, s[q]);
      m = xhalf_max(m);
      q1u[2] = hh ? 0u : (cvtpk(-m, 0.0f) & 0xffffu);
    }
    const bf16x8 q1 = __builtin_bit_cast(bf16x8, q1u);
    sa = MFMA32(k1, q1, sa);
    k0 = lds_b128(kb + 3072); k1 = lds_b128(kb + 3072 + k1off);
    bf16x8 v0 = lds_b128(vb), v1 = {};
    unsigned pp[8] = {};
    float e[4] = {};
    f32x16 o = {};
    int sc = 0;
    u64 fl = 0;
    const unsigned flo = WS_F1B + (w * 32 + r) * 128;
    __builtin_amdgcn_s_setprio(2);
    STAMP(6);
#define ATTN_STAGE(HOOK) do { \
      const int sn = sc == 2 * P2_STG ? 0 : sc + P2_STG; \
      const lds_cptr kc = kb + sc, kn = kb + sn, vc = vb + sc, vn = vb + sn; \
      attn_step<0>(HOOK, sa, sb, o, q0, q1, k0, k1, v0, v1, pp, e, kc, kn, vc, vn, k1off); \
      attn_step<1>(HOOK, sb, sa, o, q0, q1, k0, k1, v0, v1, pp, e, kc, kn, vc, vn, k1off); \
      attn_step<2>(HOOK, sa, sb, o, q0, q1, k0, k1, v0, v1, pp, e, kc, kn, vc, vn, k1off); \
      attn_step<3>(HOOK, sb, sa, o, q0, q1, k0, k1, v0, v1, pp, e, kc, kn, vc, vn, k1off); \
      attn_step<4>(HOOK, sa, sb, o, q0, q1, k0, k1, v0, v1, pp, e, kc, kn, vc, vn, k1off); \
      attn_step<5>(HOOK, sb, sa, o, q0, q1, k0, k1, v0, v1, pp, e, kc, kn, vc, vn, k1off); \
      attn_step<6>(HOOK, sa, sb, o, q0, q1, k0, k1, v0, v1, pp, e, kc, kn, vc, vn, k1off); \
      attn_step<7>(HOOK, sb, sa, o, q0, q1, k0, k1, v0, v1, pp, e, kc, kn, vc, vn, k1off); \
      scp = sc; sc = sn; } while (0)
    int scp;
    auto prio = [](int n) { if (n == 0) setprio_rt(3); if (n == 12) setprio_rt(2); if (n == 22) setprio_rt(1); };
#pragma nounroll
    for (int st = 0; st < 3; ++st) {
      ATTN_STAGE(prio);
      if (st == 2 && !__all(fl == epoch)) poll2(ws, flo, flo, epoch);
      asm volatile("s_waitcnt vmcnt(0) lgkmcnt(2)\n\ts_barrier" ::: "memory");
      glds16x3(vdma, kvg + (st + 3) * P2_STG, lds0 + scp + w * 3072);
      if (st == 1) fl = ld_flag(ws, flo);
      if (st == 0)
        glds4s(WS_O + ((qb * 8 + w) * 40 + 32 + (head >> 2)) * 1024 + ((head >> 1) & 1) * 512 + (lane & 3) * 128, ws, lds0 + P1_TCH);
      if (st == 0 && tid == 0) st_flag(ws, WS_F1B + b * 128, epoch);
      SB();
    }
    lda(0, 6 * w); lda(1, 6 * w); lda(2, 6 * w); lda(3, 6 * w); ldw(0, 6 * w);
    SB();
#pragma nounroll
    for (int st = 3; st < 8; ++st) {
      const int knx = 6 * w + st - 2;
      auto hk = [&](int n) {
        prio(n);
        if (n >= 10 && n < 30) {
          const int m = (n - 10) / 5, t = (n - 10) % 5;
          acc[m][t] = MFMA16(wf[0][t], af[m], acc[m][t]);
          if (m == 3) wf[0][t] = LD16(wofs + t * 1024, knx * 5120);
          if (t == 4) lda(m, knx);
        }
      };
      ATTN_STAGE(hk);
      if (st < 7) {
        asm volatile("s_waitcnt vmcnt(9) lgkmcnt(2)\n\ts_barrier" ::: "memory");
        if (st < 5) glds16x3(vdma, kvg + (st + 3) * P2_STG, lds0 + scp + w * 3072);
        wo_piece(2 * (st - 3)); wo_piece(2 * (st - 3) + 1);
      }
      SB();
    }
    STAMP(7);
    pp[6] = cvtpk(e[0], e[1]); pp[7] = cvtpk(e[2], e[3]);
    o = MFMA32(v1, PK4(pp[4], pp[5], pp[6], pp[7]), o);
    float l = upper_half(o[8]);
    float amax = 0.f;
#pragma unroll
    for (int q = 0; q < 12; ++q) amax = fmaxf(amax, fabsf(o[q]));
    const bool bad = !(l < 1.0e9f) || !(l > 0.25f) || !(amax < 1.0e30f);
    if (__builtin_expect(__any(bad), 0)) {
      q1u[2] = 0u;
      const bf16x8 q1z = __builtin_bit_cast(bf16x8, q1u);
      float m = -INFINITY;
      o = zero;
#pragma nounroll
      for (int t = 0; t < 64; ++t) {
        const unsigned tp = WS_KV + head * 196608 + t * 3072;
        f32x16 s = MFMA32(LD16(tp + lane * 16, 0), q0, zero);
        s = MFMA32(LD16(tp + k1off + lane * 16, 0), q1z, s);
        float tm = s[0];
#pragma unroll
        for (int q = 1; q < 16; ++q) tm = fmaxf(tm, s[q]);
        tm = xhalf_max(tm);
        const float mn = fmaxf(m, tm);
        const float al = EXP2(m - mn);
        m = mn;
        unsigned pk[8];
#pragma unroll
        for (int q = 0; q < 8; ++q) pk[q] = cvtpk(EXP2(s[2 * q] - mn), EXP2(s[2 * q + 1] - mn));
#pragma unroll
        for (int q = 0; q < 16; ++q) o[q] *= al;
        const unsigned vp = tp + 1536 + (hh * 24 + dcl) * 16;
        o = MFMA32(LD16(vp, 0), PK4(pk[0], pk[1], pk[2], pk[3]), o);
        o = MFMA32(LD16(vp + 768, 0), PK4(pk[4], pk[5], pk[6], pk[7]), o);
      }
      l = upper_half(o[8]);
    }
    const float il = 1.0f / l;
    const unsigned op = WS_O + (qb * 8 + w) * 40 * 1024;
    ST16(((u32x4){cvtpk(o[0] * il, o[1] * il), cvtpk(o[2] * il, o[3] * il), cvtpk(o[4] * il, o[5] * il), cvtpk(o[6] * il, o[7] * il)}),
         op + head * 1024 + lane * 16);
    ST8(((u32x2){cvtpk(o[8] * il, o[9] * il), cvtpk(o[10] * il, o[11] * il)}),
        hh ? OOB : op + (32 + (head >> 2)) * 1024 + (((head >> 1) & 1) * 32 + r) * 16 + (head & 1) * 8);
    STAMP(8);
  }

  {
    const int g = lane >> 4, c = lane & 15;
    LDS_AS f32x4* red = (LDS_AS f32x4*)(LDS_AS char*)lds;
    LDS_AS unsigned* cnt = (LDS_AS unsigned*)(LDS_AS char*)&lds[P1_CNT];
    wait_vm<0>();
    {
      unsigned old = 0;
      if (lane == 0) old = __hip_atomic_fetch_add(cnt, 1u, __ATOMIC_RELAXED, __HIP_MEMORY_SCOPE_WORKGROUP);
      if (lane == 0 && old == 7) {
        st_flag(ws, WS_F2 + (qb * 32 + head) * 128, epoch);
        if (b == 0) st_flag(ws, WS_SYNC, epoch);
      }
    }
    STAMP(13);
    __builtin_amdgcn_s_setprio(0);
    const int j = w & 3;
    if (w >= 4) ldw(1, 48 + 5 * j + 4);
    SB();
#pragma unroll
    for (int m = 0; m < 4; ++m)
#pragma unroll
      for (int t = 0; t < 5; ++t) acc[m][t] = MFMA16(wf[0][t], af[m], acc[m][t]);
    SB();
    u32x2 xv[3] = {}, bv[3] = {};
    auto ldres = [&]() {
#pragma unroll
      for (int u = 0; u < 3; ++u) {
        const int tl = min(w + 8 * u, 19), m = tl / 5, t = tl - m * 5;
        xv[u] = *(const u32x2*)(x + (size_t)(rb * 64 + m * 16 + c) * 640 + cq * 80 + t * 16 + 4 * g);
        bv[u] = *(const u32x2*)(b2 + cq * 80 + t * 16 + 4 * g);
      }
    };
    if (w < 4) {
      ldres();
      STAMP(14);
      lds_wait(lds0 + P1_CNT, 8u);
#pragma unroll
      for (int m = 0; m < 4; ++m)
#pragma unroll
        for (int t = 0; t < 5; ++t) red[(w * 20 + m * 5 + t) * 64 + lane] = acc[m][t];
      asm volatile("s_waitcnt lgkmcnt(0)" ::: "memory");
      __hip_atomic_store(cnt + 4 + w, 1u, __ATOMIC_RELAXED, __HIP_MEMORY_SCOPE_WORKGROUP);
      STAMP(10);
    }
    int w2 = w;
    asm volatile("" : "+s"(w2));
    if (w2 >= 4) {
      ldw(0, 48 + 5 * j + 3);
      STAMP(14);
      lds_wait(lds0 + P1_CNT + 16 + 4 * j, 1u);
#pragma unroll
      for (int m = 0; m < 4; ++m) {
#pragma unroll
        for (int t = 0; t < 5; ++t) acc[m][t] += red[(j * 20 + m * 5 + t) * 64 + lane];
        SB();
      }
      STAMP(15);
      {
        const unsigned fo = WS_F2 + ((rb >> 2) * 32 + (lane & 31)) * 128;
        poll2(ws, fo, fo, epoch);
      }
      STAMP(9);
      bf16x8 of[5][4];
#pragma unroll
      for (int k = 0; k < 5; ++k)
#pragma unroll
        for (int m = 0; m < 4; ++m) of[k][m] = LD16(ao + (m & 1) * 256, WS_O + ((2 * rb + (m >> 1)) * 40 + 2 * (5 * j + (k + 3) % 5)) * 1024);
      SB();
      const lds_cptr wol = (lds_cptr)lds + P3_WO + j * 15360 + lane * 16;
#pragma unroll
      for (int k = 0; k < 5; ++k) {
        if (k >= 1 && k < 4) {
#pragma unroll
          for (int t = 0; t < 5; ++t) wf[(k + 1) & 1][t] = lds_b128(wol + ((k - 1) * 5 + t) * 1024);
        }
        if (k == 2) ldres();
        SB();
#pragma unroll
        for (int m = 0; m < 4; ++m)
#pragma unroll
          for (int t = 0; t < 5; ++t) acc[m][t] = MFMA16(wf[k & 1][t], of[k][m], acc[m][t]);
        SB();
      }
      STAMP(10);
#pragma unroll
      for (int m = 0; m < 4; ++m)
#pragma unroll
        for (int t = 0; t < 5; ++t) red[(j * 20 + m * 5 + t) * 64 + lane] = acc[m][t];
    }
    bar_lds();
    STAMP(11);
#pragma unroll
    for (int u = 0; u < 3; ++u) {
      const int tl = w + 8 * u;
      if (tl < 20) {
        const int m = tl / 5, t = tl - m * 5;
        f32x4 a = red[tl * 64 + lane];
#pragma unroll
        for (int q = 1; q < 4; ++q) a += red[(q * 20 + tl) * 64 + lane];
        *(u32x2*)(out + (size_t)(rb * 64 + m * 16 + c) * 640 + cq * 80 + t * 16 + 4 * g) =
            (u32x2){cvtpk(a[0] + bflo(xv[u][0]) + bflo(bv[u][0]), a[1] + bfhi(xv[u][0]) + bfhi(bv[u][0])),
                    cvtpk(a[2] + bflo(xv[u][1]) + bflo(bv[u][1]), a[3] + bfhi(xv[u][1]) + bfhi(bv[u][1]))};
      }
    }
    STAMP(12);
  }
}

__global__ void code_end_marker() { asm volatile(".Lmk_end:"); }

extern "C" void kernel_launch(void* const* d_in, const int* in_sizes, int n_in, void* d_out, int out_size, void* d_ws, size_t ws_size,
                              hipStream_t stream) {
  mega_kernel<<<256, 512, 0, stream>>>((const u16*)d_in[0], (const u16*)d_in[1], (const u16*)d_in[2], (const u16*)d_in[4], (const u16*)d_in[5],
                                       (const u16*)d_in[6], (const u16*)d_in[7], (char*)d_ws, (u16*)d_out);
}
```

```cpp
#include <hip/hip_runtime.h>
#include <hip/hip_bf16.h>
#include <stdint.h>

typedef unsigned short u16;
typedef unsigned long long u64;
typedef short bf16x8 __attribute__((ext_vector_type(8)));
typedef short s16x4 __attribute__((ext_vector_type(4)));
typedef float f32x16 __attribute__((ext_vector_type(16)));
typedef float f32x4 __attribute__((ext_vector_type(4)));
typedef float f32x2 __attribute__((ext_vector_type(2)));
typedef unsigned u32x4 __attribute__((ext_vector_type(4)));
typedef unsigned u32x2 __attribute__((ext_vector_type(2)));
typedef __bf16 bf2 __attribute__((ext_vector_type(2)));
#define LDS_AS __attribute__((address_space(3)))
#define GAS __attribute__((address_space(1)))
typedef LDS_AS const char* lds_cptr;
typedef GAS u64 gu64;
#define RLX_AGENT __ATOMIC_RELAXED, __HIP_MEMORY_SCOPE_AGENT

constexpr float EPS = 1e-5f;
constexpr float QSCALE = 0.22360679774997896f * 1.4426950408889634f;

constexpr unsigned WS_SYNC = 0;
constexpr unsigned WS_F1 = 4096, WS_F1B = WS_F1 + 32768, WS_F2 = WS_F1B + 32768;
constexpr unsigned WS_Q = WS_F2 + 32768;
constexpr unsigned WS_KV = WS_Q + 32 * 64 * 1536;
constexpr unsigned WS_O = WS_KV + 32 * 196608;
constexpr unsigned WS_H = WS_O + 64 * 40 * 1024;
constexpr unsigned WS_RS = WS_H + 64 * 96 * 1024;
constexpr unsigned WS_WT = WS_RS + 32768;
constexpr unsigned WS_END = WS_WT + 8 * 68 * 5 * 1024;

#define MFMA32(a, b, c) __builtin_amdgcn_mfma_f32_32x32x16_bf16(a, b, c, 0, 0, 0)
#define MFMA16(a, b, c) __builtin_amdgcn_mfma_f32_16x16x32_bf16(a, b, c, 0, 0, 0)
#define MUL1(a, b) mulf(a, b)
#define SB() __builtin_amdgcn_sched_barrier(0)
#define EXP2(v) __builtin_amdgcn_exp2f(v)
#define PK4(a, b, c, d) __builtin_bit_cast(bf16x8, ((u32x4){a, b, c, d}))
#define ST16(v, off) __builtin_amdgcn_raw_buffer_store_b128(v, wsr, off, 0, 16)
#define ST8(v, off) __builtin_amdgcn_raw_buffer_store_b64(v, wsr, off, 0, 16)
#define LD16(off, soff) __builtin_bit_cast(bf16x8, __builtin_amdgcn_raw_buffer_load_b128(wsr, off, soff, 16))

__device__ __forceinline__ unsigned cvtpk(float lo, float hi) {
  return __builtin_bit_cast(unsigned, __builtin_convertvector((f32x2){lo, hi}, bf2));
}
__device__ __forceinline__ float bflo(unsigned u) { return __uint_as_float(u << 16); }
__device__ __forceinline__ float bfhi(unsigned u) { return __uint_as_float(u & 0xffff0000u); }
__device__ __forceinline__ void glds4(unsigned voff, const void* sbase, unsigned ldsb) {
  unsigned sv;
  asm volatile("s_mov_b32 %0, m0\n\ts_mov_b32 m0, %3\n\ts_nop 0\n\tglobal_load_lds_dword %1, %2\n\ts_mov_b32 m0, %0"
               : "=&s"(sv) : "v"(voff), "s"(sbase), "s"(ldsb) : "memory");
}
__device__ __forceinline__ void glds4s(unsigned voff, const void* sbase, unsigned ldsb) {
  unsigned sv;
  asm volatile("s_mov_b32 %0, m0\n\ts_mov_b32 m0, %3\n\ts_nop 0\n\tglobal_load_lds_dword %1, %2 sc1\n\ts_mov_b32 m0, %0"
               : "=&s"(sv) : "v"(voff), "s"(sbase), "s"(ldsb) : "memory");
}
__device__ __forceinline__ void glds16x3(unsigned voff, const void* sbase, unsigned ldsb) {
  unsigned sv;
  asm volatile("s_mov_b32 %0, m0\n\ts_mov_b32 m0, %3\n\ts_nop 0\n\t"
               "global_load_lds_dwordx4 %1, %2 sc1\n\t"
               "global_load_lds_dwordx4 %1, %2 offset:1024 sc1\n\t"
               "global_load_lds_dwordx4 %1, %2 offset:2048 sc1\n\t"
               "s_mov_b32 m0, %0"
               : "=&s"(sv) : "v"(voff), "s"(sbase), "s"(ldsb) : "memory");
}
template <int N> __device__ __forceinline__ void wait_vm() { asm volatile("s_waitcnt vmcnt(%0)" ::"i"(N) : "memory"); }
template <int N> __device__ __forceinline__ void wait_vm_bar() {
  __builtin_amdgcn_s_waitcnt(0xC07F);
  asm volatile("s_waitcnt vmcnt(%0) lgkmcnt(0)\n\ts_barrier" ::"i"(N) : "memory");
}
__device__ __forceinline__ void bar_lds() { asm volatile("s_waitcnt lgkmcnt(0)\n\ts_barrier" ::: "memory"); }
__device__ __forceinline__ bf16x8 lds_b128(lds_cptr p) { return *(const LDS_AS bf16x8*)p; }
__device__ __forceinline__ s16x4 lds_tr(lds_cptr p) { return __builtin_amdgcn_ds_read_tr16_b64_v4i16((LDS_AS s16x4*)p); }
__device__ __forceinline__ bf16x8 cat8(s16x4 a, s16x4 b) { return __builtin_shufflevector(a, b, 0, 1, 2, 3, 4, 5, 6, 7); }
__device__ __forceinline__ float xhalf_max(float v) {
  auto rr = __builtin_amdgcn_permlane32_swap(__float_as_uint(v), __float_as_uint(v), false, false);
  return fmaxf(__uint_as_float(rr[0]), __uint_as_float(rr[1]));
}
__device__ __forceinline__ float upper_half(float v) {
  auto rr = __builtin_amdgcn_permlane32_swap(__float_as_uint(v), __float_as_uint(v), false, false);
  return __uint_as_float(rr[1]);
}
template <int CTRL, int RMASK> __device__ __forceinline__ float dpp_add(float v) {
  return v + __uint_as_float(__builtin_amdgcn_update_dpp(0u, __float_as_uint(v), CTRL, RMASK, 0xf, false));
}
__device__ __forceinline__ float wave_sum(float v) {
  v = dpp_add<0x111, 0xf>(v); v = dpp_add<0x112, 0xf>(v); v = dpp_add<0x114, 0xf>(v); v = dpp_add<0x118, 0xf>(v);
  v = dpp_add<0x142, 0xa>(v); v = dpp_add<0x143, 0xc>(v);
  return __uint_as_float(__builtin_amdgcn_readlane(__float_as_uint(v), 63));
}
__device__ __forceinline__ u64 ld_flag(const char* ws, unsigned off) { return __hip_atomic_load((gu64*)(ws + off), RLX_AGENT); }
__device__ __forceinline__ void st_flag(char* ws, unsigned off, u64 v) { __hip_atomic_store((gu64*)(ws + off), v, RLX_AGENT); }
__device__ __forceinline__ u64 rfl64(u64 v) {
  return ((u64)(unsigned)__builtin_amdgcn_readfirstlane((unsigned)(v >> 32)) << 32) | (unsigned)__builtin_amdgcn_readfirstlane((unsigned)v);
}
constexpr unsigned SPIN_CAP = 1u << 18;
__device__ __forceinline__ void poll2(const char* ws, unsigned off1, unsigned off2, u64 epoch) {
  u64 a, b, m; unsigned c;
  asm volatile("s_mov_b32 %3, %8\n"
               ".Lpl%=:\n\t"
               "global_load_dwordx2 %0, %4, %6 sc1\n\t"
               "global_load_dwordx2 %1, %5, %6 sc1\n\t"
               "s_waitcnt vmcnt(0)\n\t"
               "v_cmp_ne_u64 %2, %7, %0\n\t"
               "v_cmp_ne_u64 vcc, %7, %1\n\t"
               "s_nop 1\n\t"
               "s_or_b64 %2, %2, vcc\n\t"
               "s_cbranch_scc0 .Lpd%=\n\t"
               "s_sleep 3\n\t"
               "s_sub_u32 %3, %3, 1\n\t"
               "s_cbranch_scc0 .Lpl%=\n"
               ".Lpd%=:"
               : "=&v"(a), "=&v"(b), "=&s"(m), "=&s"(c) : "v"(off1), "v"(off2), "s"(ws), "s"(epoch), "i"(SPIN_CAP) : "memory", "vcc", "scc");
}
__device__ __forceinline__ void lds_wait(unsigned la, unsigned want) {
  unsigned v, sv, c;
  asm volatile("s_mov_b32 %2, %5\n"
               ".Lwl%=:\n\t"
               "ds_read_b32 %0, %3\n\t"
               "s_waitcnt lgkmcnt(0)\n\t"
               "v_readfirstlane_b32 %1, %0\n\t"
               "s_nop 1\n\t"
               "s_cmp_ge_u32 %1, %4\n\t"
               "s_cbranch_scc1 .Lwd%=\n\t"
               "s_sleep 2\n\t"
               "s_sub_u32 %2, %2, 1\n\t"
               "s_cbranch_scc0 .Lwl%=\n"
               ".Lwd%=:"
               : "=&v"(v), "=&s"(sv), "=&s"(c) : "v"(la), "s"(want), "i"(SPIN_CAP * 8) : "memory", "scc");
}

constexpr int P1_SLOT = 32768, P1_W = 16384;
constexpr int P1_TOP = 4 * P1_SLOT;
constexpr int P1_B1 = P1_TOP + 24576, P1_CNT = P1_B1 + 256, P1_TCH = P1_CNT + 256, P1_RSV = P1_TCH + 256, P1_CSB = P1_RSV + 1536;
constexpr int LDS_BYTES = 163840;
static_assert(P1_CSB + 3072 <= LDS_BYTES, "lds");
constexpr unsigned OOB = 0x7ffffff0u;

__device__ __forceinline__ void glds16(unsigned voff, const void* sbase, unsigned ldsb) {
  unsigned sv;
  asm volatile("s_mov_b32 %0, m0\n\ts_mov_b32 m0, %3\n\ts_nop 0\n\tglobal_load_lds_dwordx4 %1, %2 sc1\n\ts_mov_b32 m0, %0"
               : "=&s"(sv) : "v"(voff), "s"(sbase), "s"(ldsb) : "memory");
}
__device__ __forceinline__ void glds16p(unsigned voff, const void* sbase, unsigned ldsb) {
  unsigned sv;
  asm volatile("s_mov_b32 %0, m0\n\ts_mov_b32 m0, %3\n\ts_nop 0\n\tglobal_load_lds_dwordx4 %1, %2\n\ts_mov_b32 m0, %0"
               : "=&s"(sv) : "v"(voff), "s"(sbase), "s"(ldsb) : "memory");
}
__device__ __forceinline__ float mulf(float a, float b) { float r; asm("v_mul_f32_e32 %0, %1, %2" : "=v"(r) : "v"(a), "v"(b)); return r; }
__device__ __forceinline__ void colsum(f32x4& cs, const bf16x8& sel, const bf16x8& f) { cs = MFMA16(sel, f, cs); SB(); }
__device__ __forceinline__ bf16x8 split_a(float v) {
  const unsigned h = cvtpk(v, 0.f) & 0xffffu, pk = h | (cvtpk(v - bflo(h), 0.f) << 16);
  return PK4(pk, pk, 0u, 0u);
}
__device__ __forceinline__ bf16x8 split_b(float v) {
  const unsigned h = cvtpk(v, v);
  const float l = v - bflo(h);
  return PK4(h, cvtpk(l, l), 0u, 0u);
}
struct P1Dma {
  unsigned vx[2], vq, vm[3];
  const char *xb[2], *qb[2], *mb[2];
  unsigned lx, lq;
};
template <int S> __device__ __forceinline__ void p1_issue(const P1Dma& d) {
  if (S < 10) {
    constexpr int so = (S & 3) * P1_SLOT;
#pragma unroll
    for (int q = 0; q < 4; ++q) {
      glds16p(d.vx[q & 1], d.xb[S & 1] + (S & ~1) * 128 + (q >> 1) * (16 * 1280), d.lx + so + q * 1024);
      glds16p(d.vq, d.qb[S & 1] + ((S & ~1) * 64 + 16 * q) * 3840, d.lq + so + P1_W + q * 4096);
    }
  } else {
    constexpr int so = S == 14 ? P1_TOP : (S & 3) * P1_SLOT;
#pragma unroll
    for (int q = 0; q < 6; ++q) glds16p(d.vm[q % 3], d.mb[q / 3] + 2 * (S - 10) * (64 * 3072), d.lq + so + (q / 3) * 12288 + (q % 3) * 4096);
  }
}
struct Corr { bf16x8 csf[2], mf[2]; };
struct FragM { bf16x8 x[2][4], w[2][4]; };
struct PtrM { lds_cptr x[2][4], w[2][2][2]; };
template <int S, int J> __device__ __forceinline__ void rd_mx(FragM& f, const PtrM& p) {
  constexpr int so = (S & 1) * P1_SLOT, H = (S >> 1) & 1;
  f.x[0][J] = lds_b128(p.x[H][J] + so); f.x[1][J] = lds_b128(p.x[H][J] + so + 4096);
}
template <int S, int J, int T> __device__ __forceinline__ void rd_mw(FragM& f, const PtrM& p) {
  constexpr int so = (S & 1) * P1_SLOT + (J >> 1) * 8192, H = (S >> 1) & 1;
  f.w[T][J] = cat8(lds_tr(p.w[H][T][J & 1] + so), lds_tr(p.w[H][T][J & 1] + so + 1024));
}
template <bool VT> __device__ __forceinline__ void mm_a(f32x16& acc, const bf16x8& xf, const bf16x8& wf) {
  SB(); acc = VT ? MFMA32(xf, wf, acc) : MFMA32(wf, xf, acc); SB();
}
template <bool VT, int S, int J>
__device__ __forceinline__ void p1_kstep_m(f32x16 (&acc)[2][2], f32x4 (&cs)[2], const bf16x8& sel, FragM& fn, const FragM& fc, const PtrM& p) {
  constexpr int N = (S + 1) % 10;
  mm_a<VT>(acc[0][0], fc.x[0][J], fc.w[0][J]); if (S < 9 && J < 3) rd_mx<N, J>(fn, p);
  mm_a<VT>(acc[1][0], fc.x[1][J], fc.w[0][J]); if (S < 9 && J < 3) rd_mw<N, J, 0>(fn, p);
  if (S < 9) colsum(cs[0], sel, fc.w[0][J]);
  mm_a<VT>(acc[1][1], fc.x[1][J], fc.w[1][J]); if (S < 9 && J < 3) rd_mw<N, J, 1>(fn, p);
  mm_a<VT>(acc[0][1], fc.x[0][J], fc.w[1][J]);
  if (S < 9 && J == 0) rd_mx<N, 3>(fn, p);
  if (S < 9 && J == 1) rd_mw<N, 3, 0>(fn, p);
  if (S < 9 && J == 2) rd_mw<N, 3, 1>(fn, p);
  if (S < 9) colsum(cs[1], sel, fc.w[1][J]);
}
template <bool VT, int S, class HK>
__device__ __forceinline__ void p1_stage_m(f32x16 (&acc)[2][2], f32x4 (&cs)[2], const bf16x8& sel, FragM& fn, const FragM& fc, const PtrM& p, Corr& cr, HK&& hook) {
  __builtin_amdgcn_s_waitcnt(0xC07F);
  if (S < 9) bar_lds();
  if (S == 9) {
    hook(89);
#pragma unroll
    for (int j = 0; j < 4; ++j) { colsum(cs[0], sel, fc.w[0][j]); colsum(cs[1], sel, fc.w[1][j]); }
  }
  p1_kstep_m<VT, S, 0>(acc, cs, sel, fn, fc, p);
  if (S == 9) hook(91);
  p1_kstep_m<VT, S, 1>(acc, cs, sel, fn, fc, p); p1_kstep_m<VT, S, 2>(acc, cs, sel, fn, fc, p);
  p1_kstep_m<VT, S, 3>(acc, cs, sel, fn, fc, p);
  if (S == 9) {
    mm_a<VT>(acc[0][0], cr.mf[0], cr.csf[0]); mm_a<VT>(acc[1][0], cr.mf[1], cr.csf[0]);
    mm_a<VT>(acc[0][1], cr.mf[0], cr.csf[1]); mm_a<VT>(acc[1][1], cr.mf[1], cr.csf[1]);
  }
  hook(S);
}
template <bool VT, class HK>
__device__ __forceinline__ void p1_pass_a(f32x16 (&acc)[2][2], f32x4 (&cs)[2], const bf16x8& sel, const PtrM& p, Corr& cr, HK&& hook) {
  FragM f0, f1;
  rd_mx<0, 0>(f0, p); rd_mw<0, 0, 0>(f0, p); rd_mw<0, 0, 1>(f0, p); rd_mx<0, 1>(f0, p); rd_mw<0, 1, 0>(f0, p); rd_mw<0, 1, 1>(f0, p);
  rd_mx<0, 2>(f0, p); rd_mw<0, 2, 0>(f0, p); rd_mw<0, 2, 1>(f0, p); rd_mx<0, 3>(f0, p); rd_mw<0, 3, 0>(f0, p); rd_mw<0, 3, 1>(f0, p);
  p1_stage_m<VT, 0>(acc, cs, sel, f1, f0, p, cr, hook); p1_stage_m<VT, 1>(acc, cs, sel, f0, f1, p, cr, hook);
  p1_stage_m<VT, 2>(acc, cs, sel, f1, f0, p, cr, hook); p1_stage_m<VT, 3>(acc, cs, sel, f0, f1, p, cr, hook);
  p1_stage_m<VT, 4>(acc, cs, sel, f1, f0, p, cr, hook); p1_stage_m<VT, 5>(acc, cs, sel, f0, f1, p, cr, hook);
  p1_stage_m<VT, 6>(acc, cs, sel, f1, f0, p, cr, hook); p1_stage_m<VT, 7>(acc, cs, sel, f0, f1, p, cr, hook);
  p1_stage_m<VT, 8>(acc, cs, sel, f1, f0, p, cr, hook); p1_stage_m<VT, 9>(acc, cs, sel, f0, f1, p, cr, hook);
}
template <int S, int N>
__device__ __forceinline__ void p1_stage_l(bf16x8 (&xk)[40], const lds_cptr (&xp)[2][4], const P1Dma& d) {
  wait_vm_bar<N>();
  if (S < 9) {
#pragma unroll
    for (int j = 0; j < 4; ++j) xk[4 * ((S + 1) % 10) + j] = lds_b128(xp[((S + 1) >> 1) & 1][j] + ((S + 1) & 1) * P1_SLOT);
  }
  SB();
  if (S < 7) p1_issue<S + 3>(d);
  if (S == 6) p1_issue<14>(d);
  if (S == 7) { p1_issue<10>(d); p1_issue<11>(d); }
}
template <int K, int J, int T> __device__ __forceinline__ bf16x8 rd_wm(const lds_cptr (&wpb)[3]) {
  constexpr int off = (K >= 8 ? P1_TOP : ((10 + K / 2) & 3) * P1_SLOT) + (K & 1) * 12288 + J * 3072 + T * 64;
  return cat8(lds_tr(wpb[off >> 16] + (off & 0xffff)), lds_tr(wpb[off >> 16] + (off & 0xffff) + 768));
}
constexpr int pbK(int n) { return n < 8 ? 8 + n / 4 : n < 16 ? n / 4 - 2 : n < 32 ? n / 4 : n / 4 - 6; }
template <int N> __device__ __forceinline__ void pb_read(bf16x8 (&g)[2][3], const lds_cptr (&wpb)[3]) {
  g[N & 1][0] = rd_wm<pbK(N), N & 3, 0>(wpb); g[N & 1][1] = rd_wm<pbK(N), N & 3, 1>(wpb); g[N & 1][2] = rd_wm<pbK(N), N & 3, 2>(wpb);
}
template <int N, int E>
__device__ __forceinline__ void pb_steps(f32x16 (&acc)[3], bf16x8 (&g)[2][3], const bf16x8 (&xk)[40], const lds_cptr (&wpb)[3]) {
  if constexpr (N < E) {
    constexpr int M = N + 2 < 40 ? N + 2 : 0;
    const bf16x8& xf = xk[4 * pbK(N) + (N & 3)];
    mm_a<false>(acc[0], xf, g[N & 1][0]); if (N + 2 < 40) g[M & 1][0] = rd_wm<pbK(M), M & 3, 0>(wpb);
    mm_a<false>(acc[1], xf, g[N & 1][1]); if (N + 2 < 40) g[M & 1][1] = rd_wm<pbK(M), M & 3, 1>(wpb);
    mm_a<false>(acc[2], xf, g[N & 1][2]); if (N + 2 < 40) g[M & 1][2] = rd_wm<pbK(M), M & 3, 2>(wpb);
    pb_steps<N + 1, E>(acc, g, xk, wpb);
  }
}
template <int K> __device__ __forceinline__ void cs_wm(f32x4 (&cs)[3], const bf16x8& sel, const lds_cptr (&wpb)[3]) {
  if constexpr (K < 10) {
    const bf16x8 a = rd_wm<K, 0, 0>(wpb), b = rd_wm<K, 0, 1>(wpb), c = rd_wm<K, 0, 2>(wpb);
    cs[0] = MFMA16(sel, a, cs[0]); cs[1] = MFMA16(sel, b, cs[1]); cs[2] = MFMA16(sel, c, cs[2]);
    cs_wm<K + 1>(cs, sel, wpb);
  }
}
__device__ __forceinline__ unsigned lds_arrive(LDS_AS unsigned* c, int lane) {
  asm volatile("s_waitcnt lgkmcnt(0)" ::: "memory");
  unsigned old = 0;
  if (lane == 0) old = __hip_atomic_fetch_add(c, 1u, __ATOMIC_RELAXED, __HIP_MEMORY_SCOPE_WORKGROUP);
  return old;
}
template <int N> __device__ __forceinline__ void vm_bar() { asm volatile("s_waitcnt vmcnt(%0)\n\ts_barrier" ::"i"(N) : "memory"); }

constexpr int P2_STG = 24576;
constexpr int P3_WO = 81920;
__device__ __forceinline__ int v_colblock(int tile) { return tile < 48 ? tile >> 2 : 12 + (tile - 48) / 3; }

#define LANES() unsigned z_ = 0; asm volatile("" : "+s"(z_)); \
  const int lane_ = __builtin_amdgcn_mbcnt_hi(~0u, __builtin_amdgcn_mbcnt_lo(~0u, z_)); \
  __builtin_assume(lane_ >= 0 && lane_ < 64); \
  const int lane = lane_, tid = w * 64 + lane, r = lane & 31, hh = lane >> 5, g4 = lane >> 4, c16 = lane & 15; (void)tid; (void)r; (void)hh; (void)g4; (void)c16
#define KV_LAMBDA() const unsigned vd2 = (unsigned)(tt * 6144 + lane * 16); \
  auto kv = [&](int st) {   \
    glds16x3(vd2, kvg + st * P2_STG, lds0 + (st % 3) * P2_STG + tt * 6144); \
    glds16x3(vd2 + 3072, kvg + st * P2_STG, lds0 + (st % 3) * P2_STG + tt * 6144 + 3072); }
#define WPB(extra) lds_cptr wpb[3]; wpb[0] = (lds_cptr)lds + (8 * hh + (c16 >> 2)) * 192 + (g4 & 1) * 32 + (lane & 3) * 8 + (extra); \
  wpb[1] = wpb[0] + 65536; wpb[2] = wpb[0] + 131072; asm volatile("" : "+v"(wpb[0]), "+v"(wpb[1]), "+v"(wpb[2]))
__global__ __launch_bounds__(512, 2) void mega_kernel(const u16* __restrict__ x, const u16* __restrict__ wqkv, const u16* __restrict__ wo,
                                                       const u16* __restrict__ w1, const u16* __restrict__ b1, const u16* __restrict__ w2,
                                                       const u16* __restrict__ b2, char* ws, u16* __restrict__ out) {
  __shared__ __attribute__((aligned(16))) char lds[LDS_BYTES];
  const unsigned lds0 = (unsigned)(uintptr_t)(LDS_AS char*)lds;
  const int b = blockIdx.x, xcd = b & 7, i = b >> 3;
  const int w = __builtin_amdgcn_readfirstlane(threadIdx.x >> 6), tt = w & 3;
  const auto wsr = __builtin_amdgcn_make_buffer_rsrc(ws, 0, WS_END, 0x00020000);
  asm volatile("" :: "s"(x), "s"(wqkv), "s"(wo), "s"(w1), "s"(b1), "s"(w2), "s"(b2), "s"(ws), "s"(out));
  const int head = xcd * 4 + (i & 3), qb = i >> 2;
  const int p = (xcd & 1) * 8 + (i & 7), cb = (xcd >> 1) * 4 + (i >> 3);
  const int id1 = p * 16 + cb;
  const int qt0 = cb < 12 ? cb * 4 : 48 + (cb - 12) * 3;
  const int nq = cb < 12 ? 4 : 3;
  const int rb = xcd * 4 + (i & 3), cq = i >> 2;
  const char* kvg = ws + WS_KV + head * 196608;
  LDS_AS f32x4* red = (LDS_AS f32x4*)(LDS_AS char*)lds;
  LDS_AS unsigned* cnt = (LDS_AS unsigned*)(LDS_AS char*)&lds[P1_CNT];
  LDS_AS float* rsv = (LDS_AS float*)(LDS_AS char*)&lds[P1_RSV];

  if (w < 4) {
    const int tp = w & 1, np = w >> 1;
    u64 epoch; unsigned tag;
    f32x16 acc[2][2] = {};
    u32x4 gr;
    bf16x8 sel;
    {
    LANES();
    {
      u64 pc; unsigned len;
      asm volatile("s_getpc_b64 %0\n.Lmk_start:\n\ts_mov_b32 %1, .Lmk_end-.Lmk_start" : "=s"(pc), "=s"(len));
      const bool isb = w == 3;
      const unsigned co = min((unsigned)((i * 3 + w) * 4 + min(lane, 3)) * 128u, len), bo = (unsigned)(cb * 192 + min(lane, 47) * 4);
      glds4(isb ? bo : co, isb ? (const char*)b1 : (const char*)pc, lds0 + (isb ? P1_B1 : P1_TCH));
    }
    const unsigned eo = ((WS_F1 + id1 * 128) & -(unsigned)(tid == 1)) | ((WS_F1B + b * 128) & -(unsigned)(tid == 2)) |
                        ((WS_F2 + (qb * 32 + head) * 128) & -(unsigned)(tid == 3)) | ((WS_RS + (p * 128 + cb * 8) * 16) & -(unsigned)(tid == 4));
    static_assert(WS_SYNC == 0, "eo = 0 selects the epoch word");
    const u64 epoch_v = ld_flag(ws, eo);
    u32x4 xa[2], xb[2];
#pragma unroll
    for (int q = 0; q < 2; ++q) {
      const u16* xr = x + (size_t)(p * 128 + cb * 8 + 2 * w + q) * 640;
      xa[q] = *(const u32x4*)(xr + lane * 8);
      xb[q] = *(const u32x4*)(xr + 512 + c16 * 8);
    }
    if (tid < 16) cnt[tid] = 0u;
    epoch = rfl64(epoch_v) + 1;
    tag = (unsigned)epoch ^ (unsigned)(epoch >> 32);
    if (tid == 0) *(LDS_AS u64*)(cnt + 16) = epoch;
#pragma unroll
    for (int q = 0; q < 2; ++q) {
      const float c = __uint_as_float(__builtin_amdgcn_readfirstlane(xa[q][0]) << 16);
      float s1 = 0.f, s2 = 0.f, t1 = 0.f, t2 = 0.f;
#pragma unroll
      for (int e = 0; e < 4; ++e) {
        const float a0 = bflo(xa[q][e]) - c, a1 = bfhi(xa[q][e]) - c, b0 = bflo(xb[q][e]) - c, b1v = bfhi(xb[q][e]) - c;
        s1 += a0 + a1; s2 = fmaf(a0, a0, fmaf(a1, a1, s2)); t1 += b0 + b1v; t2 = fmaf(b0, b0, fmaf(b1v, b1v, t2));
      }
      if (lane < 16) { s1 += t1; s2 += t2; }
      const float m = wave_sum(s1) * (1.0f / 640.0f);
      const float var = fmaxf(wave_sum(s2) * (1.0f / 640.0f) - m * m, 0.0f);
      const float r1 = __builtin_amdgcn_rsqf(var + EPS);
      const float r2 = __builtin_amdgcn_rsqf(var * r1 * r1 + EPS);
      ST16(((u32x4){__float_as_uint(m + c), __float_as_uint(r1), __float_as_uint(r1 * r2), tag}),
           lane == 0 ? WS_RS + (p * 128 + cb * 8 + 2 * w + q) * 16 : OOB);
    }

    PtrM pm;
    {
      const int q4 = c16 >> 2;
#pragma unroll
      for (int j = 0; j < 4; ++j) {
        pm.x[0][j] = (lds_cptr)lds + (tp * 64 + r) * 128 + (((2 * j + hh) ^ ((r >> 1) & 7)) << 4);
        pm.x[1][j] = pm.x[0][j] + 65536;
        asm volatile("" : "+v"(pm.x[0][j]), "+v"(pm.x[1][j]));
      }
#pragma unroll
      for (int t = 0; t < 2; ++t)
#pragma unroll
        for (int j = 0; j < 2; ++j) {
          pm.w[0][t][j] = (lds_cptr)lds + P1_W + (16 * j + 8 * hh + q4) * 256 + ((2 * np + t) ^ q4) * 64 + (g4 & 1) * 32 + (lane & 3) * 8;
          pm.w[1][t][j] = pm.w[0][t][j] + 65536;
          asm volatile("" : "+v"(pm.w[0][t][j]), "+v"(pm.w[1][t][j]));
        }
    }
    { const unsigned sv = (lane & 31) == 0 || (lane & 31) == 20 ? 0x3f803f80u : 0u; sel = PK4(sv, sv, sv, sv); }
      f32x4 cs[2] = {};
      Corr cr;
      __builtin_amdgcn_s_setprio(3);
      auto hk = [&](int S) {
        if (S != 3 && S != 5 && S < 89) return;
        LANES();
        const unsigned go = WS_RS + (p * 128 + tp * 64 + lane) * 16;
        if (S == 3)
          glds4((unsigned)(((i & 7) * 80 + w * 20 + min(lane >> 1, 19)) * 3072 + cb * 192 + (lane & 1) * 188), w1, lds0 + P1_TCH);
        if (S == 5) gr = __builtin_bit_cast(u32x4, LD16(go, 0));
        if (S == 89 && lane == 0) __hip_atomic_fetch_add(cnt + 15, 1u, __ATOMIC_RELAXED, __HIP_MEMORY_SCOPE_WORKGROUP);
        if (S == 91) {
          if (__builtin_expect(!__all(gr[3] == tag), 0)) {
#pragma nounroll
            for (unsigned sp = 0; sp < SPIN_CAP; ++sp) {
              gr = __builtin_bit_cast(u32x4, LD16(go, 0));
              if (__all(gr[3] == tag)) break;
              __builtin_amdgcn_s_sleep(3);
              asm volatile("" ::: "memory");
            }
          }
#pragma unroll
          for (int q = 0; q < 3; ++q) rsv[q * 128 + tp * 64 + lane] = __uint_as_float(gr[q]);
          const auto mn = __builtin_amdgcn_permlane32_swap(gr[0], gr[0], false, false);
#pragma unroll
          for (int t = 0; t < 2; ++t) { cr.csf[t] = split_a(cs[t][0]); cr.mf[t] = split_b(-__uint_as_float(mn[t])); }
          asm volatile("" : "+v"(cr.csf[0]), "+v"(cr.csf[1]), "+v"(cr.mf[0]), "+v"(cr.mf[1]));
        }
      };
      wait_vm<2>();
      bar_lds();
      if (cb >= 10) p1_pass_a<true>(acc, cs, sel, pm, cr, hk);
      else p1_pass_a<false>(acc, cs, sel, pm, cr, hk);
    }
    __builtin_amdgcn_s_setprio(3);
    LANES();
    KV_LAMBDA();
    {
      SB(); asm volatile("s_nop 15"); SB();
      const auto rr1 = __builtin_amdgcn_permlane32_swap(gr[1], gr[1], false, false);
      if (cb >= 10) {
#pragma unroll
        for (int a = 0; a < 2; ++a) {
          f32x4 rr[4];
#pragma unroll
          for (int g = 0; g < 4; ++g) rr[g] = *(const LDS_AS f32x4*)(rsv + 128 + tp * 64 + a * 32 + 8 * g + 4 * hh);
#pragma unroll
          for (int t = 0; t < 2; ++t) {
            const int n = (qt0 + 2 * np + t) * 32 + r - 1280;
            const int hd = __umul24(n, 3277) >> 16, dd = n - __umul24(hd, 20);
            const unsigned ov = dd == 16 ? 0x3f803f80u : 0u;
            unsigned pk[8];
#pragma unroll
            for (int g = 0; g < 4; ++g) {
              pk[2 * g] = cvtpk(MUL1(acc[a][t][4 * g], rr[g][0]), MUL1(acc[a][t][4 * g + 1], rr[g][1]));
              pk[2 * g + 1] = cvtpk(MUL1(acc[a][t][4 * g + 2], rr[g][2]), MUL1(acc[a][t][4 * g + 3], rr[g][3]));
            }
            const unsigned vp = 2 * np + t < nq ? WS_KV + __umul24(hd, 196608) + (p * 4 + 2 * tp + a) * 3072 + 1536 + (hh * 24 + dd) * 16 : OOB;
            ST16(((u32x4){pk[0], pk[1], pk[2], pk[3]}), vp);
            ST16(((u32x4){pk[4], pk[5], pk[6], pk[7]}), vp + 768);
            const unsigned vq = dd >= 16 ? vp + 64 : OOB;
            ST16(((u32x4){ov, ov, ov, ov}), vq);
            ST16(((u32x4){ov, ov, ov, ov}), vq + 768);
          }
        }
      } else {
        const bool isq = cb < 5;
        const float qs = isq ? QSCALE : 1.0f;
        const unsigned ts = isq ? 1536u : 3072u;
        const unsigned base = (isq ? WS_Q : WS_KV) + (p * 4 + 2 * tp) * ts + r * 16;
        const unsigned hs = isq ? 64u * 1536u : 196608u;
        const unsigned cst = isq ? 0u : 0x3f80u;
#pragma unroll
        for (int t = 0; t < 2; ++t)
#pragma unroll
          for (int g = 0; g < 4; g += 2) {
            const unsigned os = (qt0 + 2 * np + t) * 4 + g + hh - (isq ? 0 : 80);
            const unsigned hp = __umul24(os, 205) >> 10, oi = os - __umul24(hp, 5);
            const bool sp = oi == 2;
            const unsigned a0 = base + __umul24(2 * hp + (oi > 2), hs) + (oi > 2 ? oi - 3 : oi) * 512;
#pragma unroll
            for (int a = 0; a < 2; ++a) {
              const float rs = qs * __uint_as_float(rr1[a]);
              const f32x16& v = acc[a][t];
              const auto s0 = __builtin_amdgcn_permlane32_swap(cvtpk(MUL1(v[4 * g], rs), MUL1(v[4 * g + 1], rs)), cvtpk(MUL1(v[4 * g + 4], rs), MUL1(v[4 * g + 5], rs)), false, false);
              const auto s1 = __builtin_amdgcn_permlane32_swap(cvtpk(MUL1(v[4 * g + 2], rs), MUL1(v[4 * g + 3], rs)), cvtpk(MUL1(v[4 * g + 6], rs), MUL1(v[4 * g + 7], rs)), false, false);
              const unsigned a1 = a0 + a * ts;
              ST16(((u32x4){s0[0], s1[0], sp ? cst : s0[1], sp ? 0u : s1[1]}), a1);
              if (__any(sp)) ST16(((u32x4){s0[1], s1[1], cst, 0u}), sp ? a1 + hs : OOB);
            }
          }
      }
    }
    wait_vm<0>();
    if (lds_arrive(cnt + 8, lane) == 3 && lane == 0) st_flag(ws, WS_F1 + id1 * 128, epoch);
    {
      lds_wait(lds0 + P1_CNT + 36, 4u);
      WPB(w * 3072);
      f32x4 cs[3] = {};
      cs_wm<0>(cs, sel, wpb);
      LDS_AS float* cw = (LDS_AS float*)(LDS_AS char*)&lds[P1_CSB] + lane * 4 + w;
#pragma unroll
      for (int t = 0; t < 3; ++t) cw[t * 256] = cs[t][0];
      lds_arrive(cnt + 10, lane);
      lds_arrive(cnt + 11, lane);
    }
    if (w == 3) {
      const int ca = (head * 20) >> 7, cbb = (head * 20 + 19) >> 7;
      const int va = v_colblock(40 + ((head * 20) >> 5)), vb = v_colblock(40 + ((head * 20 + 19) >> 5));
      const int c1 = g4 == 0 ? 5 + ca : g4 == 1 ? 5 + cbb : g4 == 2 ? va : vb;
      poll2(ws, WS_F1 + (c16 * 16 + c1) * 128, WS_F1 + ((2 * qb + (lane & 1)) * 16 + ((lane & 2) ? cbb : ca)) * 128, epoch);
      lds_arrive(cnt + 10, lane);
    }
    lds_wait(lds0 + P1_CNT + 40, 9u);
    kv(0);
    bf16x8 q0[2]; u32x4 q1u[2];
    {
      const unsigned qo = WS_Q + (head * 64 + qb * 8 + 2 * w) * 1536;
#pragma unroll
      for (int g = 0; g < 2; ++g) {
        q0[g] = LD16(qo + g * 1536 + lane * 16, 0);
        q1u[g] = __builtin_bit_cast(u32x4, LD16(qo + g * 1536 + 1024 + r * 16, 0));
      }
    }
    asm volatile("" : "+v"(q0[0]), "+v"(q1u[0]), "+v"(q0[1]), "+v"(q1u[1]));
    wait_vm<0>();
    lds_arrive(cnt + 13, lane);
    kv(1);
    lds_wait(lds0 + P1_CNT + 52, 4u);
    const int dcl = r < 21 ? r : 21;
    const int k1off = hh ? 1536 + 21 * 16 - lane * 16 : 1024;
    const lds_cptr kb = (lds_cptr)lds + lane * 16;
    const f32x16 zero = {};
    const bf16x8 k0 = lds_b128(kb), k1 = lds_b128(kb + k1off);
    u32x2 q1[2];
#pragma unroll
    for (int g = 0; g < 2; ++g) {
      if (hh) q1u[g] = (u32x4){0u, 0u, 0u, 0u};
      const f32x16 s = MFMA32(k1, __builtin_bit_cast(bf16x8, q1u[g]), MFMA32(k0, q0[g], zero));
      float m = s[0];
#pragma unroll
      for (int q = 1; q < 16; ++q) m = fmaxf(m, s[q]);
      m = xhalf_max(m);
      q1[g] = (u32x2){hh ? (cvtpk(-m, 0.0f) & 0xffffu) : q1u[g][0], q1u[g][1]};
    }
    f32x16 o[2];
    asm volatile("" : "+v"(q1[0]), "+v"(q1[1]));
    wait_vm<0>();
    lds_arrive(cnt + 12, lane);
    kv(2);
    lds_wait(lds0 + P1_CNT + 48, 4u);
    __builtin_amdgcn_s_setprio(1);
    {
      unsigned st_, so_;
      asm volatile(
        "v_mov_b32 v85, %[kb]\n\t"
        "v_mov_b32 v86, %[lb]\n\t"
        "v_mov_b32 v87, %[vb]\n\t"
        "ds_read_b128 v[208:211], v85 offset:0\n\t"
        "ds_read_b64 v[216:217], v86 offset:0\n\t"
        "ds_read_b128 v[212:215], v85 offset:3072\n\t"
        "ds_read_b64 v[218:219], v86 offset:3072\n\t"
        "ds_read_b128 v[224:227], v87 offset:0\n\t"
        "v_mov_b32 v160, 0\n\t"
        "v_mov_b32 v161, 0\n\t"
        "v_mov_b32 v162, 0\n\t"
        "v_mov_b32 v163, 0\n\t"
        "v_mov_b32 v164, 0\n\t"
        "v_mov_b32 v165, 0\n\t"
        "v_mov_b32 v166, 0\n\t"
        "v_mov_b32 v167, 0\n\t"
        "v_mov_b32 v168, 0\n\t"
        "v_mov_b32 v169, 0\n\t"
        "v_mov_b32 v170, 0\n\t"
        "v_mov_b32 v171, 0\n\t"
        "v_mov_b32 v172, 0\n\t"
        "v_mov_b32 v173, 0\n\t"
        "v_mov_b32 v174, 0\n\t"
        "v_mov_b32 v175, 0\n\t"
        "v_mov_b32 v176, 0\n\t"
        "v_mov_b32 v177, 0\n\t"
        "v_mov_b32 v178, 0\n\t"
        "v_mov_b32 v179, 0\n\t"
        "v_mov_b32 v180, 0\n\t"
        "v_mov_b32 v181, 0\n\t"
        "v_mov_b32 v182, 0\n\t"
        "v_mov_b32 v183, 0\n\t"
        "v_mov_b32 v184, 0\n\t"
        "v_mov_b32 v185, 0\n\t"
        "v_mov_b32 v186, 0\n\t"
        "v_mov_b32 v187, 0\n\t"
        "v_mov_b32 v188, 0\n\t"
        "v_mov_b32 v189, 0\n\t"
        "v_mov_b32 v190, 0\n\t"
        "v_mov_b32 v191, 0\n\t"
        "v_mov_b32 v236, 0\n\t"
        "v_mov_b32 v237, 0\n\t"
        "v_mov_b32 v238, 0\n\t"
        "v_mov_b32 v239, 0\n\t"
        "v_mov_b32 v240, 0\n\t"
        "v_mov_b32 v241, 0\n\t"
        "v_mov_b32 v242, 0\n\t"
        "v_mov_b32 v243, 0\n\t"
        "v_mov_b32 v244, 0\n\t"
        "v_mov_b32 v245, 0\n\t"
        "v_mov_b32 v246, 0\n\t"
        "v_mov_b32 v247, 0\n\t"
        "v_mov_b32 v248, 0\n\t"
        "v_mov_b32 v249, 0\n\t"
        "v_mov_b32 v250, 0\n\t"
        "v_mov_b32 v251, 0\n\t"
        "v_mov_b32 v252, 0\n\t"
        "v_mov_b32 v253, 0\n\t"
        "v_mov_b32 v254, 0\n\t"
        "v_mov_b32 v255, 0\n\t"
        "v_mov_b32 v88, 0\n\t"
        "v_mov_b32 v89, 0\n\t"
        "v_mov_b32 v90, 0\n\t"
        "v_mov_b32 v91, 0\n\t"
        "v_mov_b32 v92, 0\n\t"
        "v_mov_b32 v93, 0\n\t"
        "v_mov_b32 v94, 0\n\t"
        "v_mov_b32 v95, 0\n\t"
        "s_mov_b32 %[st], 0\n\t"
        "s_mov_b32 %[so], 0\n\t"
        "s_waitcnt lgkmcnt(0)\n\t"
        "v_mfma_f32_32x32x16_bf16 v[96:111], v[208:211], v[192:195], 0\n\t"
        "v_mfma_f32_32x32x16_bf16 v[112:127], v[208:211], v[200:203], 0\n\t"
        "v_mfma_f32_32x32x8bf16_1k v[96:111], v[216:217], v[196:197], v[96:111]\n\t"
        "v_mfma_f32_32x32x8bf16_1k v[112:127], v[216:217], v[204:205], v[112:127]\n\t"
        "s_nop 7\n\t"
        ".Lat%=:\n\t"
        "s_add_u32 %[so], %[so], 0x6000\n\t"
        "s_cmp_lg_u32 %[so], 0x12000\n\t"
        "s_cselect_b32 %[so], %[so], 0\n\t"
        "v_mov_b32 v82, v85\n\t"
        "v_mov_b32 v83, v86\n\t"
        "v_mov_b32 v84, v87\n\t"
        "v_add_u32 v85, %[so], %[kb]\n\t"
        "v_add_u32 v86, %[so], %[lb]\n\t"
        "v_add_u32 v87, %[so], %[vb]\n\t"
        "s_waitcnt lgkmcnt(0)\n\t"
        "v_mfma_f32_32x32x16_bf16 v[128:143], v[212:215], v[192:195], 0\n\t"
        "ds_read_b128 v[232:235], v84 offset:768\n\t"
        "v_cvt_pk_bf16_f32 v94, v252, v253\n\t"
        "v_cvt_pk_bf16_f32 v95, v254, v255\n\t"
        "v_exp_f32_e32 v248, v96\n\t"
        "v_exp_f32_e32 v249, v97\n\t"
        "v_exp_f32_e32 v250, v98\n\t"
        "v_exp_f32_e32 v251, v99\n\t"
        "v_mfma_f32_32x32x16_bf16 v[144:159], v[212:215], v[200:203], 0\n\t"
        "ds_read_b128 v[208:211], v82 offset:6144\n\t"
        "v_cvt_pk_bf16_f32 v240, v248, v249\n\t"
        "v_cvt_pk_bf16_f32 v241, v250, v251\n\t"
        "v_exp_f32_e32 v252, v100\n\t"
        "v_exp_f32_e32 v253, v101\n\t"
        "v_exp_f32_e32 v254, v102\n\t"
        "v_exp_f32_e32 v255, v103\n\t"
        "v_mfma_f32_32x32x16_bf16 v[160:175], v[236:239], v[244:247], v[160:175]\n\t"
        "ds_read_b64 v[216:217], v83 offset:6144\n\t"
        "v_cvt_pk_bf16_f32 v242, v252, v253\n\t"
        "v_cvt_pk_bf16_f32 v243, v254, v255\n\t"
        "v_exp_f32_e32 v248, v112\n\t"
        "v_exp_f32_e32 v249, v113\n\t"
        "v_exp_f32_e32 v250, v114\n\t"
        "v_exp_f32_e32 v251, v115\n\t"
        "v_mfma_f32_32x32x16_bf16 v[176:191], v[236:239], v[92:95], v[176:191]\n\t"
        "ds_read_b128 v[228:231], v84 offset:3072\n\t"
        "v_cvt_pk_bf16_f32 v88, v248, v249\n\t"
        "v_cvt_pk_bf16_f32 v89, v250, v251\n\t"
        "v_exp_f32_e32 v252, v116\n\t"
        "v_exp_f32_e32 v253, v117\n\t"
        "v_exp_f32_e32 v254, v118\n\t"
        "v_exp_f32_e32 v255, v119\n\t"
        "v_mfma_f32_32x32x8bf16_1k v[128:143], v[218:219], v[196:197], v[128:143]\n\t"
        "v_cvt_pk_bf16_f32 v90, v252, v253\n\t"
        "v_cvt_pk_bf16_f32 v91, v254, v255\n\t"
        "v_exp_f32_e32 v248, v104\n\t"
        "v_exp_f32_e32 v249, v105\n\t"
        "v_exp_f32_e32 v250, v106\n\t"
        "v_exp_f32_e32 v251, v107\n\t"
        "v_mfma_f32_32x32x8bf16_1k v[144:159], v[218:219], v[204:205], v[144:159]\n\t"
        "v_cvt_pk_bf16_f32 v244, v248, v249\n\t"
        "v_cvt_pk_bf16_f32 v245, v250, v251\n\t"
        "v_exp_f32_e32 v252, v108\n\t"
        "v_exp_f32_e32 v253, v109\n\t"
        "v_exp_f32_e32 v254, v110\n\t"
        "v_exp_f32_e32 v255, v111\n\t"
        "v_mfma_f32_32x32x16_bf16 v[160:175], v[224:227], v[240:243], v[160:175]\n\t"
        "v_cvt_pk_bf16_f32 v246, v252, v253\n\t"
        "v_cvt_pk_bf16_f32 v247, v254, v255\n\t"
        "v_exp_f32_e32 v248, v120\n\t"
        "v_exp_f32_e32 v249, v121\n\t"
        "v_exp_f32_e32 v250, v122\n\t"
        "v_exp_f32_e32 v251, v123\n\t"
        "v_mfma_f32_32x32x16_bf16 v[176:191], v[224:227], v[88:91], v[176:191]\n\t"
        "v_cvt_pk_bf16_f32 v92, v248, v249\n\t"
        "v_cvt_pk_bf16_f32 v93, v250, v251\n\t"
        "v_exp_f32_e32 v252, v124\n\t"
        "v_exp_f32_e32 v253, v125\n\t"
        "v_exp_f32_e32 v254, v126\n\t"
        "v_exp_f32_e32 v255, v127\n\t"
        "s_waitcnt lgkmcnt(0)\n\t"
        "v_mfma_f32_32x32x16_bf16 v[96:111], v[208:211], v[192:195], 0\n\t"
        "ds_read_b128 v[236:239], v84 offset:3840\n\t"
        "v_cvt_pk_bf16_f32 v94, v252, v253\n\t"
        "v_cvt_pk_bf16_f32 v95, v254, v255\n\t"
        "v_exp_f32_e32 v248, v128\n\t"
        "v_exp_f32_e32 v249, v129\n\t"
        "v_exp_f32_e32 v250, v130\n\t"
        "v_exp_f32_e32 v251, v131\n\t"
        "v_mfma_f32_32x32x16_bf16 v[112:127], v[208:211], v[200:203], 0\n\t"
        "ds_read_b128 v[212:215], v82 offset:9216\n\t"
        "v_cvt_pk_bf16_f32 v240, v248, v249\n\t"
        "v_cvt_pk_bf16_f32 v241, v250, v251\n\t"
        "v_exp_f32_e32 v252, v132\n\t"
        "v_exp_f32_e32 v253, v133\n\t"
        "v_exp_f32_e32 v254, v134\n\t"
        "v_exp_f32_e32 v255, v135\n\t"
        "v_mfma_f32_32x32x16_bf16 v[160:175], v[232:235], v[244:247], v[160:175]\n\t"
        "ds_read_b64 v[218:219], v83 offset:9216\n\t"
        "v_cvt_pk_bf16_f32 v242, v252, v253\n\t"
        "v_cvt_pk_bf16_f32 v243, v254, v255\n\t"
        "v_exp_f32_e32 v248, v144\n\t"
        "v_exp_f32_e32 v249, v145\n\t"
        "v_exp_f32_e32 v250, v146\n\t"
        "v_exp_f32_e32 v251, v147\n\t"
        "v_mfma_f32_32x32x16_bf16 v[176:191], v[232:235], v[92:95], v[176:191]\n\t"
        "ds_read_b128 v[224:227], v84 offset:6144\n\t"
        "v_cvt_pk_bf16_f32 v88, v248, v249\n\t"
        "v_cvt_pk_bf16_f32 v89, v250, v251\n\t"
        "v_exp_f32_e32 v252, v148\n\t"
        "v_exp_f32_e32 v253, v149\n\t"
        "v_exp_f32_e32 v254, v150\n\t"
        "v_exp_f32_e32 v255, v151\n\t"
        "v_mfma_f32_32x32x8bf16_1k v[96:111], v[216:217], v[196:197], v[96:111]\n\t"
        "v_cvt_pk_bf16_f32 v90, v252, v253\n\t"
        "v_cvt_pk_bf16_f32 v91, v254, v255\n\t"
        "v_exp_f32_e32 v248, v136\n\t"
        "v_exp_f32_e32 v249, v137\n\t"
        "v_exp_f32_e32 v250, v138\n\t"
        "v_exp_f32_e32 v251, v139\n\t"
        "v_mfma_f32_32x32x8bf16_1k v[112:127], v[216:217], v[204:205], v[112:127]\n\t"
        "v_cvt_pk_bf16_f32 v244, v248, v249\n\t"
        "v_cvt_pk_bf16_f32 v245, v250, v251\n\t"
        "v_exp_f32_e32 v252, v140\n\t"
        "v_exp_f32_e32 v253, v141\n\t"
        "v_exp_f32_e32 v254, v142\n\t"
        "v_exp_f32_e32 v255, v143\n\t"
        "v_mfma_f32_32x32x16_bf16 v[160:175], v[228:231], v[240:243], v[160:175]\n\t"
        "v_cvt_pk_bf16_f32 v246, v252, v253\n\t"
        "v_cvt_pk_bf16_f32 v247, v254, v255\n\t"
        "v_exp_f32_e32 v248, v152\n\t"
        "v_exp_f32_e32 v249, v153\n\t"
        "v_exp_f32_e32 v250, v154\n\t"
        "v_exp_f32_e32 v251, v155\n\t"
        "v_mfma_f32_32x32x16_bf16 v[176:191], v[228:231], v[88:91], v[176:191]\n\t"
        "v_cvt_pk_bf16_f32 v92, v248, v249\n\t"
        "v_cvt_pk_bf16_f32 v93, v250, v251\n\t"
        "v_exp_f32_e32 v252, v156\n\t"
        "v_exp_f32_e32 v253, v157\n\t"
        "v_exp_f32_e32 v254, v158\n\t"
        "v_exp_f32_e32 v255, v159\n\t"
        "s_waitcnt lgkmcnt(0)\n\t"
        "v_mfma_f32_32x32x16_bf16 v[128:143], v[212:215], v[192:195], 0\n\t"
        "ds_read_b128 v[232:235], v84 offset:6912\n\t"
        "v_cvt_pk_bf16_f32 v94, v252, v253\n\t"
        "v_cvt_pk_bf16_f32 v95, v254, v255\n\t"
        "v_exp_f32_e32 v248, v96\n\t"
        "v_exp_f32_e32 v249, v97\n\t"
        "v_exp_f32_e32 v250, v98\n\t"
        "v_exp_f32_e32 v251, v99\n\t"
        "v_mfma_f32_32x32x16_bf16 v[144:159], v[212:215], v[200:203], 0\n\t"
        "ds_read_b128 v[208:211], v82 offset:12288\n\t"
        "v_cvt_pk_bf16_f32 v240, v248, v249\n\t"
        "v_cvt_pk_bf16_f32 v241, v250, v251\n\t"
        "v_exp_f32_e32 v252, v100\n\t"
        "v_exp_f32_e32 v253, v101\n\t"
        "v_exp_f32_e32 v254, v102\n\t"
        "v_exp_f32_e32 v255, v103\n\t"
        "v_mfma_f32_32x32x16_bf16 v[160:175], v[236:239], v[244:247], v[160:175]\n\t"
        "ds_read_b64 v[216:217], v83 offset:12288\n\t"
        "v_cvt_pk_bf16_f32 v242, v252, v253\n\t"
        "v_cvt_pk_bf16_f32 v243, v254, v255\n\t"
        "v_exp_f32_e32 v248, v112\n\t"
        "v_exp_f32_e32 v249, v113\n\t"
        "v_exp_f32_e32 v250, v114\n\t"
        "v_exp_f32_e32 v251, v115\n\t"
        "v_mfma_f32_32x32x16_bf16 v[176:191], v[236:239], v[92:95], v[176:191]\n\t"
        "ds_read_b128 v[228:231], v84 offset:9216\n\t"
        "v_cvt_pk_bf16_f32 v88, v248, v249\n\t"
        "v_cvt_pk_bf16_f32 v89, v250, v251\n\t"
        "v_exp_f32_e32 v252, v116\n\t"
        "v_exp_f32_e32 v253, v117\n\t"
        "v_exp_f32_e32 v254, v118\n\t"
        "v_exp_f32_e32 v255, v119\n\t"
        "v_mfma_f32_32x32x8bf16_1k v[128:143], v[218:219], v[196:197], v[128:143]\n\t"
        "v_cvt_pk_bf16_f32 v90, v252, v253\n\t"
        "v_cvt_pk_bf16_f32 v91, v254, v255\n\t"
        "v_exp_f32_e32 v248, v104\n\t"
        "v_exp_f32_e32 v249, v105\n\t"
        "v_exp_f32_e32 v250, v106\n\t"
        "v_exp_f32_e32 v251, v107\n\t"
        "v_mfma_f32_32x32x8bf16_1k v[144:159], v[218:219], v[204:205], v[144:159]\n\t"
        "v_cvt_pk_bf16_f32 v244, v248, v249\n\t"
        "v_cvt_pk_bf16_f32 v245, v250, v251\n\t"
        "v_exp_f32_e32 v252, v108\n\t"
        "v_exp_f32_e32 v253, v109\n\t"
        "v_exp_f32_e32 v254, v110\n\t"
        "v_exp_f32_e32 v255, v111\n\t"
        "v_mfma_f32_32x32x16_bf16 v[160:175], v[224:227], v[240:243], v[160:175]\n\t"
        "v_cvt_pk_bf16_f32 v246, v252, v253\n\t"
        "v_cvt_pk_bf16_f32 v247, v254, v255\n\t"
        "v_exp_f32_e32 v248, v120\n\t"
        "v_exp_f32_e32 v249, v121\n\t"
        "v_exp_f32_e32 v250, v122\n\t"
        "v_exp_f32_e32 v251, v123\n\t"
        "v_mfma_f32_32x32x16_bf16 v[176:191], v[224:227], v[88:91], v[176:191]\n\t"
        "v_cvt_pk_bf16_f32 v92, v248, v249\n\t"
        "v_cvt_pk_bf16_f32 v93, v250, v251\n\t"
        "v_exp_f32_e32 v252, v124\n\t"
        "v_exp_f32_e32 v253, v125\n\t"
        "v_exp_f32_e32 v254, v126\n\t"
        "v_exp_f32_e32 v255, v127\n\t"
        "s_waitcnt lgkmcnt(0)\n\t"
        "v_mfma_f32_32x32x16_bf16 v[96:111], v[208:211], v[192:195], 0\n\t"
        "ds_read_b128 v[236:239], v84 offset:9984\n\t"
        "v_cvt_pk_bf16_f32 v94, v252, v253\n\t"
        "v_cvt_pk_bf16_f32 v95, v254, v255\n\t"
        "v_exp_f32_e32 v248, v128\n\t"
        "v_exp_f32_e32 v249, v129\n\t"
        "v_exp_f32_e32 v250, v130\n\t"
        "v_exp_f32_e32 v251, v131\n\t"
        "v_mfma_f32_32x32x16_bf16 v[112:127], v[208:211], v[200:203], 0\n\t"
        "ds_read_b128 v[212:215], v82 offset:15360\n\t"
        "v_cvt_pk_bf16_f32 v240, v248, v249\n\t"
        "v_cvt_pk_bf16_f32 v241, v250, v251\n\t"
        "v_exp_f32_e32 v252, v132\n\t"
        "v_exp_f32_e32 v253, v133\n\t"
        "v_exp_f32_e32 v254, v134\n\t"
        "v_exp_f32_e32 v255, v135\n\t"
        "v_mfma_f32_32x32x16_bf16 v[160:175], v[232:235], v[244:247], v[160:175]\n\t"
        "ds_read_b64 v[218:219], v83 offset:15360\n\t"
        "v_cvt_pk_bf16_f32 v242, v252, v253\n\t"
        "v_cvt_pk_bf16_f32 v243, v254, v255\n\t"
        "v_exp_f32_e32 v248, v144\n\t"
        "v_exp_f32_e32 v249, v145\n\t"
        "v_exp_f32_e32 v250, v146\n\t"
        "v_exp_f32_e32 v251, v147\n\t"
        "v_mfma_f32_32x32x16_bf16 v[176:191], v[232:235], v[92:95], v[176:191]\n\t"
        "ds_read_b128 v[224:227], v84 offset:12288\n\t"
        "v_cvt_pk_bf16_f32 v88, v248, v249\n\t"
        "v_cvt_pk_bf16_f32 v89, v250, v251\n\t"
        "v_exp_f32_e32 v252, v148\n\t"
        "v_exp_f32_e32 v253, v149\n\t"
        "v_exp_f32_e32 v254, v150\n\t"
        "v_exp_f32_e32 v255, v151\n\t"
        "v_mfma_f32_32x32x8bf16_1k v[96:111], v[216:217], v[196:197], v[96:111]\n\t"
        "v_cvt_pk_bf16_f32 v90, v252, v253\n\t"
        "v_cvt_pk_bf16_f32 v91, v254, v255\n\t"
        "v_exp_f32_e32 v248, v136\n\t"
        "v_exp_f32_e32 v249, v137\n\t"
        "v_exp_f32_e32 v250, v138\n\t"
        "v_exp_f32_e32 v251, v139\n\t"
        "v_mfma_f32_32x32x8bf16_1k v[112:127], v[216:217], v[204:205], v[112:127]\n\t"
        "v_cvt_pk_bf16_f32 v244, v248, v249\n\t"
        "v_cvt_pk_bf16_f32 v245, v250, v251\n\t"
        "v_exp_f32_e32 v252, v140\n\t"
        "v_exp_f32_e32 v253, v141\n\t"
        "v_exp_f32_e32 v254, v142\n\t"
        "v_exp_f32_e32 v255, v143\n\t"
        "v_mfma_f32_32x32x16_bf16 v[160:175], v[228:231], v[240:243], v[160:175]\n\t"
        "v_cvt_pk_bf16_f32 v246, v252, v253\n\t"
        "v_cvt_pk_bf16_f32 v247, v254, v255\n\t"
        "v_exp_f32_e32 v248, v152\n\t"
        "v_exp_f32_e32 v249, v153\n\t"
        "v_exp_f32_e32 v250, v154\n\t"
        "v_exp_f32_e32 v251, v155\n\t"
        "v_mfma_f32_32x32x16_bf16 v[176:191], v[228:231], v[88:91], v[176:191]\n\t"
        "v_cvt_pk_bf16_f32 v92, v248, v249\n\t"
        "v_cvt_pk_bf16_f32 v93, v250, v251\n\t"
        "v_exp_f32_e32 v252, v156\n\t"
        "v_exp_f32_e32 v253, v157\n\t"
        "v_exp_f32_e32 v254, v158\n\t"
        "v_exp_f32_e32 v255, v159\n\t"
        "s_waitcnt lgkmcnt(0)\n\t"
        "v_mfma_f32_32x32x16_bf16 v[128:143], v[212:215], v[192:195], 0\n\t"
        "ds_read_b128 v[232:235], v84 offset:13056\n\t"
        "v_cvt_pk_bf16_f32 v94, v252, v253\n\t"
        "v_cvt_pk_bf16_f32 v95, v254, v255\n\t"
        "v_exp_f32_e32 v248, v96\n\t"
        "v_exp_f32_e32 v249, v97\n\t"
        "v_exp_f32_e32 v250, v98\n\t"
        "v_exp_f32_e32 v251, v99\n\t"
        "v_mfma_f32_32x32x16_bf16 v[144:159], v[212:215], v[200:203], 0\n\t"
        "ds_read_b128 v[208:211], v82 offset:18432\n\t"
        "v_cvt_pk_bf16_f32 v240, v248, v249\n\t"
        "v_cvt_pk_bf16_f32 v241, v250, v251\n\t"
        "v_exp_f32_e32 v252, v100\n\t"
        "v_exp_f32_e32 v253, v101\n\t"
        "v_exp_f32_e32 v254, v102\n\t"
        "v_exp_f32_e32 v255, v103\n\t"
        "v_mfma_f32_32x32x16_bf16 v[160:175], v[236:239], v[244:247], v[160:175]\n\t"
        "ds_read_b64 v[216:217], v83 offset:18432\n\t"
        "v_cvt_pk_bf16_f32 v242, v252, v253\n\t"
        "v_cvt_pk_bf16_f32 v243, v254, v255\n\t"
        "v_exp_f32_e32 v248, v112\n\t"
        "v_exp_f32_e32 v249, v113\n\t"
        "v_exp_f32_e32 v250, v114\n\t"
        "v_exp_f32_e32 v251, v115\n\t"
        "v_mfma_f32_32x32x16_bf16 v[176:191], v[236:239], v[92:95], v[176:191]\n\t"
        "ds_read_b128 v[228:231], v84 offset:15360\n\t"
        "v_cvt_pk_bf16_f32 v88, v248, v249\n\t"
        "v_cvt_pk_bf16_f32 v89, v250, v251\n\t"
        "v_exp_f32_e32 v252, v116\n\t"
        "v_exp_f32_e32 v253, v117\n\t"
        "v_exp_f32_e32 v254, v118\n\t"
        "v_exp_f32_e32 v255, v119\n\t"
        "v_mfma_f32_32x32x8bf16_1k v[128:143], v[218:219], v[196:197], v[128:143]\n\t"
        "v_cvt_pk_bf16_f32 v90, v252, v253\n\t"
        "v_cvt_pk_bf16_f32 v91, v254, v255\n\t"
        "v_exp_f32_e32 v248, v104\n\t"
        "v_exp_f32_e32 v249, v105\n\t"
        "v_exp_f32_e32 v250, v106\n\t"
        "v_exp_f32_e32 v251, v107\n\t"
        "v_mfma_f32_32x32x8bf16_1k v[144:159], v[218:219], v[204:205], v[144:159]\n\t"
        "v_cvt_pk_bf16_f32 v244, v248, v249\n\t"
        "v_cvt_pk_bf16_f32 v245, v250, v251\n\t"
        "v_exp_f32_e32 v252, v108\n\t"
        "v_exp_f32_e32 v253, v109\n\t"
        "v_exp_f32_e32 v254, v110\n\t"
        "v_exp_f32_e32 v255, v111\n\t"
        "v_mfma_f32_32x32x16_bf16 v[160:175], v[224:227], v[240:243], v[160:175]\n\t"
        "v_cvt_pk_bf16_f32 v246, v252, v253\n\t"
        "v_cvt_pk_bf16_f32 v247, v254, v255\n\t"
        "v_exp_f32_e32 v248, v120\n\t"
        "v_exp_f32_e32 v249, v121\n\t"
        "v_exp_f32_e32 v250, v122\n\t"
        "v_exp_f32_e32 v251, v123\n\t"
        "v_mfma_f32_32x32x16_bf16 v[176:191], v[224:227], v[88:91], v[176:191]\n\t"
        "v_cvt_pk_bf16_f32 v92, v248, v249\n\t"
        "v_cvt_pk_bf16_f32 v93, v250, v251\n\t"
        "v_exp_f32_e32 v252, v124\n\t"
        "v_exp_f32_e32 v253, v125\n\t"
        "v_exp_f32_e32 v254, v126\n\t"
        "v_exp_f32_e32 v255, v127\n\t"
        "s_waitcnt lgkmcnt(0)\n\t"
        "v_mfma_f32_32x32x16_bf16 v[96:111], v[208:211], v[192:195], 0\n\t"
        "ds_read_b128 v[236:239], v84 offset:16128\n\t"
        "v_cvt_pk_bf16_f32 v94, v252, v253\n\t"
        "v_cvt_pk_bf16_f32 v95, v254, v255\n\t"
        "v_exp_f32_e32 v248, v128\n\t"
        "v_exp_f32_e32 v249, v129\n\t"
        "v_exp_f32_e32 v250, v130\n\t"
        "v_exp_f32_e32 v251, v131\n\t"
        "v_mfma_f32_32x32x16_bf16 v[112:127], v[208:211], v[200:203], 0\n\t"
        "ds_read_b128 v[212:215], v82 offset:21504\n\t"
        "v_cvt_pk_bf16_f32 v240, v248, v249\n\t"
        "v_cvt_pk_bf16_f32 v241, v250, v251\n\t"
        "v_exp_f32_e32 v252, v132\n\t"
        "v_exp_f32_e32 v253, v133\n\t"
        "v_exp_f32_e32 v254, v134\n\t"
        "v_exp_f32_e32 v255, v135\n\t"
        "v_mfma_f32_32x32x16_bf16 v[160:175], v[232:235], v[244:247], v[160:175]\n\t"
        "ds_read_b64 v[218:219], v83 offset:21504\n\t"
        "v_cvt_pk_bf16_f32 v242, v252, v253\n\t"
        "v_cvt_pk_bf16_f32 v243, v254, v255\n\t"
        "v_exp_f32_e32 v248, v144\n\t"
        "v_exp_f32_e32 v249, v145\n\t"
        "v_exp_f32_e32 v250, v146\n\t"
        "v_exp_f32_e32 v251, v147\n\t"
        "v_mfma_f32_32x32x16_bf16 v[176:191], v[232:235], v[92:95], v[176:191]\n\t"
        "ds_read_b128 v[224:227], v84 offset:18432\n\t"
        "v_cvt_pk_bf16_f32 v88, v248, v249\n\t"
        "v_cvt_pk_bf16_f32 v89, v250, v251\n\t"
        "v_exp_f32_e32 v252, v148\n\t"
        "v_exp_f32_e32 v253, v149\n\t"
        "v_exp_f32_e32 v254, v150\n\t"
        "v_exp_f32_e32 v255, v151\n\t"
        "v_mfma_f32_32x32x8bf16_1k v[96:111], v[216:217], v[196:197], v[96:111]\n\t"
        "v_cvt_pk_bf16_f32 v90, v252, v253\n\t"
        "v_cvt_pk_bf16_f32 v91, v254, v255\n\t"
        "v_exp_f32_e32 v248, v136\n\t"
        "v_exp_f32_e32 v249, v137\n\t"
        "v_exp_f32_e32 v250, v138\n\t"
        "v_exp_f32_e32 v251, v139\n\t"
        "v_mfma_f32_32x32x8bf16_1k v[112:127], v[216:217], v[204:205], v[112:127]\n\t"
        "v_cvt_pk_bf16_f32 v244, v248, v249\n\t"
        "v_cvt_pk_bf16_f32 v245, v250, v251\n\t"
        "v_exp_f32_e32 v252, v140\n\t"
        "v_exp_f32_e32 v253, v141\n\t"
        "v_exp_f32_e32 v254, v142\n\t"
        "v_exp_f32_e32 v255, v143\n\t"
        "v_mfma_f32_32x32x16_bf16 v[160:175], v[228:231], v[240:243], v[160:175]\n\t"
        "v_cvt_pk_bf16_f32 v246, v252, v253\n\t"
        "v_cvt_pk_bf16_f32 v247, v254, v255\n\t"
        "v_exp_f32_e32 v248, v152\n\t"
        "v_exp_f32_e32 v249, v153\n\t"
        "v_exp_f32_e32 v250, v154\n\t"
        "v_exp_f32_e32 v251, v155\n\t"
        "v_mfma_f32_32x32x16_bf16 v[176:191], v[228:231], v[88:91], v[176:191]\n\t"
        "v_cvt_pk_bf16_f32 v92, v248, v249\n\t"
        "v_cvt_pk_bf16_f32 v93, v250, v251\n\t"
        "v_exp_f32_e32 v252, v156\n\t"
        "v_exp_f32_e32 v253, v157\n\t"
        "v_exp_f32_e32 v254, v158\n\t"
        "v_exp_f32_e32 v255, v159\n\t"
        "s_waitcnt lgkmcnt(0)\n\t"
        "v_mfma_f32_32x32x16_bf16 v[128:143], v[212:215], v[192:195], 0\n\t"
        "ds_read_b128 v[232:235], v84 offset:19200\n\t"
        "v_cvt_pk_bf16_f32 v94, v252, v253\n\t"
        "v_cvt_pk_bf16_f32 v95, v254, v255\n\t"
        "v_exp_f32_e32 v248, v96\n\t"
        "v_exp_f32_e32 v249, v97\n\t"
        "v_exp_f32_e32 v250, v98\n\t"
        "v_exp_f32_e32 v251, v99\n\t"
        "v_mfma_f32_32x32x16_bf16 v[144:159], v[212:215], v[200:203], 0\n\t"
        "ds_read_b128 v[208:211], v85 offset:0\n\t"
        "v_cvt_pk_bf16_f32 v240, v248, v249\n\t"
        "v_cvt_pk_bf16_f32 v241, v250, v251\n\t"
        "v_exp_f32_e32 v252, v100\n\t"
        "v_exp_f32_e32 v253, v101\n\t"
        "v_exp_f32_e32 v254, v102\n\t"
        "v_exp_f32_e32 v255, v103\n\t"
        "v_mfma_f32_32x32x16_bf16 v[160:175], v[236:239], v[244:247], v[160:175]\n\t"
        "ds_read_b64 v[216:217], v86 offset:0\n\t"
        "v_cvt_pk_bf16_f32 v242, v252, v253\n\t"
        "v_cvt_pk_bf16_f32 v243, v254, v255\n\t"
        "v_exp_f32_e32 v248, v112\n\t"
        "v_exp_f32_e32 v249, v113\n\t"
        "v_exp_f32_e32 v250, v114\n\t"
        "v_exp_f32_e32 v251, v115\n\t"
        "v_mfma_f32_32x32x16_bf16 v[176:191], v[236:239], v[92:95], v[176:191]\n\t"
        "ds_read_b128 v[228:231], v84 offset:21504\n\t"
        "v_cvt_pk_bf16_f32 v88, v248, v249\n\t"
        "v_cvt_pk_bf16_f32 v89, v250, v251\n\t"
        "v_exp_f32_e32 v252, v116\n\t"
        "v_exp_f32_e32 v253, v117\n\t"
        "v_exp_f32_e32 v254, v118\n\t"
        "v_exp_f32_e32 v255, v119\n\t"
        "v_mfma_f32_32x32x8bf16_1k v[128:143], v[218:219], v[196:197], v[128:143]\n\t"
        "v_cvt_pk_bf16_f32 v90, v252, v253\n\t"
        "v_cvt_pk_bf16_f32 v91, v254, v255\n\t"
        "v_exp_f32_e32 v248, v104\n\t"
        "v_exp_f32_e32 v249, v105\n\t"
        "v_exp_f32_e32 v250, v106\n\t"
        "v_exp_f32_e32 v251, v107\n\t"
        "v_mfma_f32_32x32x8bf16_1k v[144:159], v[218:219], v[204:205], v[144:159]\n\t"
        "v_cvt_pk_bf16_f32 v244, v248, v249\n\t"
        "v_cvt_pk_bf16_f32 v245, v250, v251\n\t"
        "v_exp_f32_e32 v252, v108\n\t"
        "v_exp_f32_e32 v253, v109\n\t"
        "v_exp_f32_e32 v254, v110\n\t"
        "v_exp_f32_e32 v255, v111\n\t"
        "v_mfma_f32_32x32x16_bf16 v[160:175], v[224:227], v[240:243], v[160:175]\n\t"
        "v_cvt_pk_bf16_f32 v246, v252, v253\n\t"
        "v_cvt_pk_bf16_f32 v247, v254, v255\n\t"
        "v_exp_f32_e32 v248, v120\n\t"
        "v_exp_f32_e32 v249, v121\n\t"
        "v_exp_f32_e32 v250, v122\n\t"
        "v_exp_f32_e32 v251, v123\n\t"
        "v_mfma_f32_32x32x16_bf16 v[176:191], v[224:227], v[88:91], v[176:191]\n\t"
        "v_cvt_pk_bf16_f32 v92, v248, v249\n\t"
        "v_cvt_pk_bf16_f32 v93, v250, v251\n\t"
        "v_exp_f32_e32 v252, v124\n\t"
        "v_exp_f32_e32 v253, v125\n\t"
        "v_exp_f32_e32 v254, v126\n\t"
        "v_exp_f32_e32 v255, v127\n\t"
        "s_waitcnt lgkmcnt(0)\n\t"
        "v_mfma_f32_32x32x16_bf16 v[96:111], v[208:211], v[192:195], 0\n\t"
        "ds_read_b128 v[236:239], v84 offset:22272\n\t"
        "v_cvt_pk_bf16_f32 v94, v252, v253\n\t"
        "v_cvt_pk_bf16_f32 v95, v254, v255\n\t"
        "v_exp_f32_e32 v248, v128\n\t"
        "v_exp_f32_e32 v249, v129\n\t"
        "v_exp_f32_e32 v250, v130\n\t"
        "v_exp_f32_e32 v251, v131\n\t"
        "v_mfma_f32_32x32x16_bf16 v[112:127], v[208:211], v[200:203], 0\n\t"
        "ds_read_b128 v[212:215], v85 offset:3072\n\t"
        "v_cvt_pk_bf16_f32 v240, v248, v249\n\t"
        "v_cvt_pk_bf16_f32 v241, v250, v251\n\t"
        "v_exp_f32_e32 v252, v132\n\t"
        "v_exp_f32_e32 v253, v133\n\t"
        "v_exp_f32_e32 v254, v134\n\t"
        "v_exp_f32_e32 v255, v135\n\t"
        "v_mfma_f32_32x32x16_bf16 v[160:175], v[232:235], v[244:247], v[160:175]\n\t"
        "ds_read_b64 v[218:219], v86 offset:3072\n\t"
        "v_cvt_pk_bf16_f32 v242, v252, v253\n\t"
        "v_cvt_pk_bf16_f32 v243, v254, v255\n\t"
        "v_exp_f32_e32 v248, v144\n\t"
        "v_exp_f32_e32 v249, v145\n\t"
        "v_exp_f32_e32 v250, v146\n\t"
        "v_exp_f32_e32 v251, v147\n\t"
        "v_mfma_f32_32x32x16_bf16 v[176:191], v[232:235], v[92:95], v[176:191]\n\t"
        "ds_read_b128 v[224:227], v87 offset:0\n\t"
        "v_cvt_pk_bf16_f32 v88, v248, v249\n\t"
        "v_cvt_pk_bf16_f32 v89, v250, v251\n\t"
        "v_exp_f32_e32 v252, v148\n\t"
        "v_exp_f32_e32 v253, v149\n\t"
        "v_exp_f32_e32 v254, v150\n\t"
        "v_exp_f32_e32 v255, v151\n\t"
        "v_mfma_f32_32x32x8bf16_1k v[96:111], v[216:217], v[196:197], v[96:111]\n\t"
        "v_cvt_pk_bf16_f32 v90, v252, v253\n\t"
        "v_cvt_pk_bf16_f32 v91, v254, v255\n\t"
        "v_exp_f32_e32 v248, v136\n\t"
        "v_exp_f32_e32 v249, v137\n\t"
        "v_exp_f32_e32 v250, v138\n\t"
        "v_exp_f32_e32 v251, v139\n\t"
        "v_mfma_f32_32x32x8bf16_1k v[112:127], v[216:217], v[204:205], v[112:127]\n\t"
        "v_cvt_pk_bf16_f32 v244, v248, v249\n\t"
        "v_cvt_pk_bf16_f32 v245, v250, v251\n\t"
        "v_exp_f32_e32 v252, v140\n\t"
        "v_exp_f32_e32 v253, v141\n\t"
        "v_exp_f32_e32 v254, v142\n\t"
        "v_exp_f32_e32 v255, v143\n\t"
        "v_mfma_f32_32x32x16_bf16 v[160:175], v[228:231], v[240:243], v[160:175]\n\t"
        "v_cvt_pk_bf16_f32 v246, v252, v253\n\t"
        "v_cvt_pk_bf16_f32 v247, v254, v255\n\t"
        "v_exp_f32_e32 v248, v152\n\t"
        "v_exp_f32_e32 v249, v153\n\t"
        "v_exp_f32_e32 v250, v154\n\t"
        "v_exp_f32_e32 v251, v155\n\t"
        "v_mfma_f32_32x32x16_bf16 v[176:191], v[228:231], v[88:91], v[176:191]\n\t"
        "v_cvt_pk_bf16_f32 v92, v248, v249\n\t"
        "v_cvt_pk_bf16_f32 v93, v250, v251\n\t"
        "v_exp_f32_e32 v252, v156\n\t"
        "v_exp_f32_e32 v253, v157\n\t"
        "v_exp_f32_e32 v254, v158\n\t"
        "v_exp_f32_e32 v255, v159\n\t"
        "s_waitcnt lgkmcnt(0)\n\t"
        "s_add_u32 %[st], %[st], 1\n\t"
        "s_cmp_gt_u32 %[st], 6\n\t"
        "s_cbranch_scc1 .Lan%=\n\t"
        "s_waitcnt vmcnt(0)\n\t"
        "s_barrier\n\t"
        ".Lan%=:\n\t"
        "s_cmp_lt_u32 %[st], 8\n\t"
        "s_cbranch_scc1 .Lat%=\n\t"
        "v_cvt_pk_bf16_f32 v94, v252, v253\n\t"
        "v_cvt_pk_bf16_f32 v95, v254, v255\n\t"
        "v_mfma_f32_32x32x16_bf16 v[160:175], v[236:239], v[244:247], v[160:175]\n\t"
        "s_nop 0\n\t"
        "v_mfma_f32_32x32x16_bf16 v[176:191], v[236:239], v[92:95], v[176:191]\n\t"
        "s_waitcnt lgkmcnt(0)\n\t"
        "s_nop 15\n\t"
        : "=&{v[160:175]}"(o[0]), "=&{v[176:191]}"(o[1]), [st] "=&s"(st_), [so] "=&s"(so_)
        : "{v[192:195]}"(q0[0]), "{v[196:197]}"(q1[0]), "{v[200:203]}"(q0[1]), "{v[204:205]}"(q1[1]),
          [kb] "v"(lds0 + lane * 16), [lb] "v"(lds0 + 1024 + r * 16 + hh * 8), [vb] "v"(lds0 + 1536 + (hh * 24 + dcl) * 16)
        : "memory", "scc", "v82", "v83", "v84", "v85", "v86", "v87", "v88", "v89", "v90", "v91", "v92", "v93", "v94", "v95", "v96", "v97", "v98", "v99", "v100", "v101", "v102", "v103", "v104", "v105", "v106", "v107", "v108", "v109", "v110", "v111", "v112", "v113", "v114", "v115", "v116", "v117", "v118", "v119", "v120", "v121", "v122", "v123", "v124", "v125", "v126", "v127", "v128", "v129", "v130", "v131", "v132", "v133", "v134", "v135", "v136", "v137", "v138", "v139", "v140", "v141", "v142", "v143", "v144", "v145", "v146", "v147", "v148", "v149", "v150", "v151", "v152", "v153", "v154", "v155", "v156", "v157", "v158", "v159", "v208", "v209", "v210", "v211", "v212", "v213", "v214", "v215", "v216", "v217", "v218", "v219", "v220", "v221", "v222", "v223", "v224", "v225", "v226", "v227", "v228", "v229", "v230", "v231", "v232", "v233", "v234", "v235", "v236", "v237", "v238", "v239", "v240", "v241", "v242", "v243", "v244", "v245", "v246", "v247", "v248", "v249", "v250", "v251", "v252", "v253", "v254", "v255");
    }
#pragma unroll
    for (int g = 0; g < 2; ++g) {
      float l = upper_half(o[g][8]);
      float amax = 0.f;
#pragma unroll
      for (int q = 0; q < 12; ++q) amax = fmaxf(amax, fabsf(o[g][q]));
      const bool bad = !(l < 1.0e9f) || !(l > 0.25f) || !(amax < 1.0e30f);
      if (__builtin_expect(__any(bad), 0)) {
        q1u[g][2] = 0u;
        const bf16x8 q1z = __builtin_bit_cast(bf16x8, q1u[g]);
        float m = -INFINITY;
        o[g] = zero;
#pragma nounroll
        for (int t = 0; t < 64; ++t) {
          const unsigned tp = WS_KV + head * 196608 + t * 3072;
          f32x16 s = MFMA32(LD16(tp + lane * 16, 0), q0[g], zero);
          s = MFMA32(LD16(tp + k1off + lane * 16, 0), q1z, s);
          float tm = s[0];
#pragma unroll
          for (int q = 1; q < 16; ++q) tm = fmaxf(tm, s[q]);
          tm = xhalf_max(tm);
          const float mn = fmaxf(m, tm);
          const float al = EXP2(m - mn);
          m = mn;
          unsigned pk[8];
#pragma unroll
          for (int q = 0; q < 8; ++q) pk[q] = cvtpk(EXP2(s[2 * q] - mn), EXP2(s[2 * q + 1] - mn));
#pragma unroll
          for (int q = 0; q < 16; ++q) o[g][q] *= al;
          const unsigned vp = tp + 1536 + (hh * 24 + dcl) * 16;
          o[g] = MFMA32(LD16(vp, 0), PK4(pk[0], pk[1], pk[2], pk[3]), o[g]);
          o[g] = MFMA32(LD16(vp + 768, 0), PK4(pk[4], pk[5], pk[6], pk[7]), o[g]);
        }
        l = upper_half(o[g][8]);
      }
      const float il = 1.0f / l;
      const unsigned op = WS_O + (qb * 8 + 2 * w + g) * 40 * 1024;
      ST16(((u32x4){cvtpk(o[g][0] * il, o[g][1] * il), cvtpk(o[g][2] * il, o[g][3] * il), cvtpk(o[g][4] * il, o[g][5] * il), cvtpk(o[g][6] * il, o[g][7] * il)}),
           op + head * 1024 + lane * 16);
      ST8(((u32x2){cvtpk(o[g][8] * il, o[g][9] * il), cvtpk(o[g][10] * il, o[g][11] * il)}),
          hh ? OOB : op + (32 + (head >> 2)) * 1024 + (((head >> 1) & 1) * 32 + r) * 16 + (head & 1) * 8);
    }
    SB();
    __hip_atomic_store(cnt + 4 + w, 1u, __ATOMIC_RELAXED, __HIP_MEMORY_SCOPE_WORKGROUP);
    __builtin_amdgcn_s_setprio(3);
    wait_vm<0>();
    unsigned old = 0;
    if (lane == 0) old = __hip_atomic_fetch_add(cnt, 1u, __ATOMIC_RELAXED, __HIP_MEMORY_SCOPE_WORKGROUP);
    if (lane == 0 && old == 3) st_flag(ws, WS_F2 + (qb * 32 + head) * 128, epoch);
    __builtin_amdgcn_s_setprio(0);
    f32x4 res[5];
#pragma unroll
    for (int t = 0; t < 5; ++t) {
      const u32x2 xv = *(const u32x2*)(x + (size_t)(rb * 64 + w * 16 + c16) * 640 + cq * 80 + t * 16 + 4 * g4);
      const u32x2 bv = *(const u32x2*)(b2 + cq * 80 + t * 16 + 4 * g4);
      res[t] = (f32x4){bflo(xv[0]) + bflo(bv[0]), bfhi(xv[0]) + bfhi(bv[0]), bflo(xv[1]) + bflo(bv[1]), bfhi(xv[1]) + bfhi(bv[1])};
    }
    asm volatile("" : "+v"(res[0]), "+v"(res[1]), "+v"(res[2]), "+v"(res[3]), "+v"(res[4]));
    bar_lds();
    u16* op = out + (size_t)(rb * 64 + w * 16 + c16) * 640 + cq * 80 + 4 * g4;
    f32x4 pr[5][4];
#pragma unroll
    for (int t = 0; t < 5; ++t)
#pragma unroll
      for (int q = 0; q < 4; ++q) pr[t][q] = red[(q * 20 + w * 5 + t) * 64 + lane];
    SB();
#pragma unroll
    for (int t = 0; t < 5; ++t) {
      const f32x4 a = ((pr[t][0] + pr[t][1]) + (pr[t][2] + pr[t][3])) + res[t];
      *(u32x2*)(op + t * 16) = (u32x2){cvtpk(a[0], a[1]), cvtpk(a[2], a[3])};
    }
  }
  int wv = w;
  asm volatile("" : "+s"(wv));
  if (wv >= 4) {
    const int j = tt;
    {
      LANES();
      P1Dma d;
      const int kp = ((i >> 3) ^ i) & 1;
#pragma unroll
      for (int q = 0; q < 2; ++q) {
        const int row = 32 * j + 8 * q + (lane >> 3);
        d.vx[q] = (unsigned)((p * 128 + row) * 1280 + (((lane & 7) ^ ((row >> 1) & 7)) << 4));
        d.xb[q] = (const char*)x + (q ^ kp) * 128;
        d.qb[q] = (const char*)wqkv + (q ^ kp) * (64 * 3840);
        d.mb[q] = (const char*)w1 + (q ^ kp) * (64 * 3072);
      }
      const int qr = 4 * j + g4;
      d.vq = (unsigned)(qr * 3840 + (qt0 + min((((lane >> 2) & 3) ^ (qr & 3)), nq - 1)) * 64 + (lane & 3) * 16);
      d.lx = lds0 + j * 4096; d.lq = lds0 + j * 1024;
      p1_issue<0>(d); p1_issue<1>(d);
#pragma unroll
      for (int q = 0; q < 3; ++q) { const int u = q * 256 + j * 64 + lane; d.vm[q] = (unsigned)((u / 12) * 3072 + cb * 192 + (u % 12) * 16); }
      lds_cptr xp[2][4];
#pragma unroll
      for (int q = 0; q < 4; ++q) {
        xp[0][q] = (lds_cptr)lds + (j * 32 + r) * 128 + (((2 * q + hh) ^ ((r >> 1) & 7)) << 4);
        xp[1][q] = xp[0][q] + 65536;
        asm volatile("" : "+v"(xp[0][q]), "+v"(xp[1][q]));
      }
      bf16x8 xk[40];
      vm_bar<8>();
#pragma unroll
      for (int q = 0; q < 4; ++q) xk[q] = lds_b128(xp[0][q]);
      SB();
      p1_issue<2>(d);
      p1_stage_l<0, 8>(xk, xp, d);  p1_stage_l<1, 8>(xk, xp, d); p1_stage_l<2, 8>(xk, xp, d); p1_stage_l<3, 8>(xk, xp, d);
      p1_stage_l<4, 8>(xk, xp, d); p1_stage_l<5, 8>(xk, xp, d); p1_stage_l<6, 8>(xk, xp, d); p1_stage_l<7, 14>(xk, xp, d);
      p1_stage_l<8, 18>(xk, xp, d);
      lds_arrive(cnt + 15, lane);
      p1_issue<12>(d);
      lds_wait(lds0 + P1_CNT + 60, 8u);
      p1_issue<13>(d);
      f32x16 ac[3] = {};
      bf16x8 g[2][3];
      WPB(0);
      wait_vm<18>();
      lds_arrive(cnt + 14, lane);
      lds_wait(lds0 + P1_CNT + 56, 4u);
      pb_read<0>(g, wpb); pb_read<1>(g, wpb);
      pb_steps<0, 10>(ac, g, xk, wpb);
      wait_vm<0>();
      if (lane == 0) __hip_atomic_fetch_add(cnt + 9, 1u, __ATOMIC_RELAXED, __HIP_MEMORY_SCOPE_WORKGROUP);
      pb_steps<10, 14>(ac, g, xk, wpb);
      lds_wait(lds0 + P1_CNT + 36, 4u);
      pb_steps<14, 30>(ac, g, xk, wpb);
      lds_arrive(cnt + 10, lane);
      pb_steps<30, 40>(ac, g, xk, wpb);
      lds_wait(lds0 + P1_CNT + 44, 4u);
      const bf16x8 mk = split_b(-rsv[j * 32 + r]);
      const float rs = rsv[256 + j * 32 + r];
#pragma unroll
      for (int t = 0; t < 3; ++t) {
        const f32x4 cc = *(const LDS_AS f32x4*)(LDS_AS char*)&lds[P1_CSB + (t * 64 + lane) * 16];
        ac[t] = MFMA32(split_a((cc[0] + cc[1]) + (cc[2] + cc[3])), mk, ac[t]);
      }
      const unsigned hp = WS_H + ((p * 4 + j) * 96 + cb * 6) * 1024 + lane * 16;
      u32x4 hs[6];
      const float gc0 = -2.0f * 0.7978845608028654f * 1.4426950408889634f, gc1 = gc0 * 0.044715f;
      __builtin_amdgcn_s_setprio(2);
      SB(); asm volatile("s_nop 15"); SB();
#pragma unroll
      for (int t = 0; t < 3; ++t)
#pragma unroll
        for (int hf = 0; hf < 2; ++hf) {
          unsigned pk[4];
#pragma unroll
          for (int u = 0; u < 2; ++u) {
            const u32x2 bq = *(const u32x2*)(lds + P1_B1 + (t * 32 + 16 * hf + 8 * u + 4 * hh) * 2);
            const int e = 8 * hf + 4 * u;
            float gx[4], gz[4];
            asm("v_lshlrev_b32_e32 %4, 16, %12\n\tv_and_b32_e32 %5, 0xffff0000, %12\n\tv_lshlrev_b32_e32 %6, 16, %13\n\tv_and_b32_e32 %7, 0xffff0000, %13\n\t"
                "v_fma_f32 %0, %8, %14, %4\n\tv_fma_f32 %1, %9, %14, %5\n\tv_fma_f32 %2, %10, %14, %6\n\tv_fma_f32 %3, %11, %14, %7\n\t"
                "v_mul_f32_e32 %4, %0, %0\n\tv_mul_f32_e32 %5, %1, %1\n\tv_mul_f32_e32 %6, %2, %2\n\tv_mul_f32_e32 %7, %3, %3\n\t"
                "v_fma_f32 %4, %4, %15, %16\n\tv_fma_f32 %5, %5, %15, %16\n\tv_fma_f32 %6, %6, %15, %16\n\tv_fma_f32 %7, %7, %15, %16\n\t"
                "v_mul_f32_e32 %4, %0, %4\n\tv_mul_f32_e32 %5, %1, %5\n\tv_mul_f32_e32 %6, %2, %6\n\tv_mul_f32_e32 %7, %3, %7\n\t"
                "v_exp_f32_e32 %4, %4\n\tv_exp_f32_e32 %5, %5\n\tv_exp_f32_e32 %6, %6\n\tv_exp_f32_e32 %7, %7\n\t"
                "v_add_f32_e32 %4, 1.0, %4\n\tv_add_f32_e32 %5, 1.0, %5\n\tv_add_f32_e32 %6, 1.0, %6\n\tv_add_f32_e32 %7, 1.0, %7\n\t"
                "v_rcp_f32_e32 %4, %4\n\tv_rcp_f32_e32 %5, %5\n\tv_rcp_f32_e32 %6, %6\n\tv_rcp_f32_e32 %7, %7\n\t"
                "v_mul_f32_e32 %4, %0, %4\n\tv_mul_f32_e32 %5, %1, %5\n\tv_mul_f32_e32 %6, %2, %6\n\tv_mul_f32_e32 %7, %3, %7"
                : "=&v"(gx[0]), "=&v"(gx[1]), "=&v"(gx[2]), "=&v"(gx[3]), "=&v"(gz[0]), "=&v"(gz[1]), "=&v"(gz[2]), "=&v"(gz[3])
                : "v"(ac[t][e]), "v"(ac[t][e + 1]), "v"(ac[t][e + 2]), "v"(ac[t][e + 3]), "v"(bq[0]), "v"(bq[1]), "v"(rs), "s"(gc1), "v"(gc0));
            pk[2 * u] = cvtpk(gz[0], gz[1]); pk[2 * u + 1] = cvtpk(gz[2], gz[3]);
          }
          hs[2 * t + hf] = (u32x4){pk[0], pk[1], pk[2], pk[3]};
        }
      asm volatile("" : "+v"(hs[0]), "+v"(hs[1]), "+v"(hs[2]), "+v"(hs[3]), "+v"(hs[4]), "+v"(hs[5]));
      lds_wait(lds0 + P1_CNT + 48, 4u);
#pragma unroll
      for (int q = 0; q < 6; ++q) ST16(hs[q], hp + q * 1024);
    }
    LANES();
    KV_LAMBDA();
    const unsigned ao = (lane >> 5) * 1024 + (((lane >> 4) & 1) * 32 + (lane & 15)) * 16;
    const unsigned wofs = WS_WT + cq * 68 * 5 * 1024 + lane * 16;
    bf16x8 af[3][4], wf[3][5];
    f32x4 acc[4][5] = {};
    auto ldw = [&](int s, int kk) {
#pragma unroll
      for (int t = 0; t < 5; ++t) wf[s][t] = LD16(wofs + t * 1024, kk * 5120);
    };
    auto ldh = [&](int s, int kk) {
#pragma unroll
      for (int m = 0; m < 4; ++m) af[s][m] = LD16(ao + (m & 1) * 256, WS_H + ((2 * rb + (m >> 1)) * 96 + 2 * kk) * 1024);
      ldw(s, kk);
    };
    auto mm = [&](int s, int pace) {
#pragma unroll
      for (int m = 0; m < 4; ++m)
#pragma unroll
        for (int t = 0; t < 5; ++t) {
          SB();
          acc[m][t] = MFMA16(wf[s][t], af[s][m], acc[m][t]);
          if (pace && ((m * 5 + t) % (pace ? pace : 1)) == pace - 1) __builtin_amdgcn_s_sleep(1);
        }
      SB();
    };
    const u64 epoch = rfl64(*(LDS_AS u64*)(cnt + 16));
    __builtin_amdgcn_s_setprio(2);
    const int bk = j * 256 + b;
    if (bk < 544) {
      const int kk = bk % 68, c8 = bk / 68;
      const int r0 = kk < 48 ? 32 * kk : kk < 64 ? 40 * (kk - 48) : 160 * (kk - 64) + 16;
      const int ra = kk < 48 ? 16 : kk < 64 ? 20 : 80, rbb = kk < 64 ? 8 : 20, rc = kk < 64 ? 4 : 40;
#pragma unroll
      for (int q = 0; q < 5; ++q) {
        const int u = q * 64 + lane, rho = u / 10;
        const int row = r0 + ra * (rho >> 4) + rc * ((rho >> 3) & 1) + rbb * ((rho >> 2) & 1) + (rho & 3);
        glds16p((unsigned)(row * 1280 + c8 * 160 + (u % 10) * 16), kk < 48 ? w2 : wo, lds0 + P1_TOP + j * 5120 + q * 1024);
      }
    }
    glds4s(WS_O + ((qb * 8 + 2 * j + ((lane >> 2) & 1)) * 40 + 32 + (head >> 2)) * 1024 + ((head >> 1) & 1) * 512 + (lane & 3) * 128, ws, lds0 + P1_TCH);
    asm volatile("s_barrier" ::: "memory");
    kv(3);
    if (bk < 544) {
      wait_vm<6>();
      const lds_cptr rp = (lds_cptr)lds + P1_TOP + j * 5120 + (8 * g4 + (c16 >> 2)) * 160 + (lane & 3) * 8;
#pragma unroll
      for (int t = 0; t < 5; ++t)
        ST16(__builtin_bit_cast(u32x4, cat8(lds_tr(rp + t * 32), lds_tr(rp + t * 32 + 640))), WS_WT + (bk * 5 + t) * 1024 + lane * 16);
    }
    vm_bar<0>();
    if (tid == 256) st_flag(ws, WS_F1B + b * 128, epoch);
    kv(4);
    vm_bar<0>();
    kv(5);
    __builtin_amdgcn_s_sleep(24);
    poll2(ws, WS_F1B + (j * 64 + lane) * 128, WS_F1B + (j * 64 + lane) * 128, epoch);
    asm volatile("s_barrier" ::: "memory");
    if (b == 0 && tid == 256) st_flag(ws, WS_SYNC, epoch);
    kv(6);
    const int k0h = 12 * j;
    ldh(0, k0h); ldh(1, k0h + 1);
    mm(0, 2); ldh(2, k0h + 2);
    vm_bar<27>();
    kv(7);
    ldh(0, k0h + 3);
    mm(1, 2); ldh(1, k0h + 4);
    mm(2, 2);
    vm_bar<18>();
    ldh(2, k0h + 5);
    mm(0, 2); ldh(0, k0h + 6);
    mm(1, 2); ldh(1, k0h + 7);
#pragma unroll
    for (int q = 0; q < 15; ++q) glds16(wofs, ws + (48 + 5 * j) * 5120 + q * 1024, lds0 + P3_WO + (j * 15 + q) * 1024);
    mm(2, 2); ldh(2, k0h + 8);
    mm(0, 2); ldh(0, k0h + 9);
    mm(1, 2); ldh(1, k0h + 10);
    lds_wait(lds0 + P1_CNT + 16 + 4 * j, 1u);
    mm(2, 0); ldh(2, k0h + 11);
    mm(0, 0); ldw(0, 48 + 5 * j + 3);
    mm(1, 0); ldw(1, 48 + 5 * j + 4);
    mm(2, 0);
    lds_wait(lds0 + P1_CNT, 4u);
    {
      const unsigned fo = WS_F2 + ((rb >> 2) * 32 + (lane & 31)) * 128;
      poll2(ws, fo, fo, epoch);
    }
    bf16x8 of[5][4];
#pragma unroll
    for (int k = 0; k < 5; ++k)
#pragma unroll
      for (int m = 0; m < 4; ++m) of[k][m] = LD16(ao + (m & 1) * 256, WS_O + ((2 * rb + (m >> 1)) * 40 + 2 * (5 * j + (k + 3) % 5)) * 1024);
    SB();
    const lds_cptr wol = (lds_cptr)lds + P3_WO + j * 15360 + lane * 16;
#pragma unroll
    for (int k = 0; k < 5; ++k) {
      if (k >= 1 && k < 4) {
#pragma unroll
        for (int t = 0; t < 5; ++t) wf[(k + 1) & 1][t] = lds_b128(wol + ((k - 1) * 5 + t) * 1024);
      }
      SB();
#pragma unroll
      for (int m = 0; m < 4; ++m)
#pragma unroll
        for (int t = 0; t < 5; ++t) {
          acc[m][t] = MFMA16(wf[k & 1][t], of[k][m], acc[m][t]);
          if (k == 4 && m > 0) { red[(j * 20 + (m - 1) * 5 + t) * 64 + lane] = acc[m - 1][t]; SB(); }
        }
      SB();
    }
#pragma unroll
    for (int t = 0; t < 5; ++t) red[(j * 20 + 15 + t) * 64 + lane] = acc[3][t];
    bar_lds();
  }
}

__global__ void code_end_marker() { asm volatile(".Lmk_end:"); }

extern "C" void kernel_launch(void* const* d_in, const int* in_sizes, int n_in, void* d_out, int out_size, void* d_ws, size_t ws_size,
                              hipStream_t stream) {
  mega_kernel<<<256, 512, 0, stream>>>((const u16*)d_in[0], (const u16*)d_in[1], (const u16*)d_in[2], (const u16*)d_in[4], (const u16*)d_in[5],
                                       (const u16*)d_in[6], (const u16*)d_in[7], (char*)d_ws, (u16*)d_out);
}
```
